```python
import torch

def _softmax(x, axis=-1):
    return torch.softmax(x, dim=axis)
def _gelu(x):
    return torch.nn.functional.gelu(x, approximate="tanh")
def _silu(x):
    return torch.nn.functional.silu(x)
def _sigmoid(x):
    return torch.sigmoid(x)
def _layernorm(x, axis=-1, eps=1e-5):
    m = x.mean(dim=axis, keepdim=True)
    v = x.var(dim=axis, keepdim=True, unbiased=False)
    return (x - m) / torch.sqrt(v + eps)
def _scatter_add(tgt, idx, src, axis=0):
    return tgt.clone().index_add_(axis, idx, src)
def _segment_sum(src, seg, n):
    out = torch.zeros((n,) + src.shape[1:], dtype=src.dtype)
    return out.index_add_(0, seg, src)
def _pad(x, pw):
    flat = tuple(v for pair in reversed(pw) for v in pair)
    return torch.nn.functional.pad(x, flat)

kernel_name = 'interleave_layers__gqa_block_cached__compressed_kv_cla__decode'

def setup_inputs():

    _w = (lambda a: (lambda t: t.to(torch.bfloat16) if t.is_floating_point() else t)(torch.as_tensor(a)))
    def _A_setup():
        B, Sq, D = 1, 1, 640
        Hq, Hkv, Sk, Dh = 32, 32, 4096, 192
        assert Hq % Hkv == 0, (Hq, Hkv)
        return {
            "x": _w((lambda *s: torch.randn(*s, dtype=torch.bfloat16) * 0.05)(B, Sq, D)),
            "k_cache": _w((lambda *s: torch.randn(*s, dtype=torch.bfloat16) * 0.8)(B, Hkv, Sk, Dh)),
            "v_cache": _w((lambda *s: torch.randn(*s, dtype=torch.bfloat16) * 0.8)(B, Hkv, Sk, Dh)),
            "wq": _w((lambda *s: torch.randn(*s, dtype=torch.bfloat16) * 0.8)(D, Hq * Dh) * (D ** -0.5)),
            "wo": _w((lambda *s: torch.randn(*s, dtype=torch.bfloat16) * 0.8)(Hq * Dh, D) * ((Hq * Dh) ** -0.5)),
        }
    _A_in = _A_setup()

    def _B_setup():
        B, H, Sq, Sk, D, M = 1, 32, 1, 4096, 192, 640
        Dc = 4 * D
        return {
            "x": _w((lambda *s: torch.randn(*s, dtype=torch.bfloat16) * 0.05)(B, Sq, M)),
            "c_kv": _w((lambda *s: torch.randn(*s, dtype=torch.bfloat16) * 0.4)(B, Sk, Dc)),
            "w_q": _w((lambda *s: torch.randn(*s, dtype=torch.bfloat16) * 3.2)(M, H, D) * (M ** -0.5)),
            "w_uk": _w((lambda *s: torch.randn(*s, dtype=torch.bfloat16) * 3.2)(Dc, H, D) * (Dc ** -0.5)),
            "w_uv": _w((lambda *s: torch.randn(*s, dtype=torch.bfloat16) * 0.4)(Dc, H, D) * (Dc ** -0.5)),
            "w_o": _w((lambda *s: torch.randn(*s, dtype=torch.bfloat16) * 0.4)(H, D, M) * ((H * D) ** -0.5)),
        }
    _B_in = _B_setup()

    return {
        "A_x": _A_in["x"],
        "A_k_cache": _A_in["k_cache"],
        "A_v_cache": _A_in["v_cache"],
        "A_wq": _A_in["wq"],
        "A_wo": _A_in["wo"],
        "B_x": _B_in["x"],
        "B_c_kv": _B_in["c_kv"],
        "B_w_q": _B_in["w_q"],
        "B_w_uk": _B_in["w_uk"],
        "B_w_uv": _B_in["w_uv"],
        "B_w_o": _B_in["w_o"],
    }

def reference(A_x, A_k_cache, A_v_cache, A_wq, A_wo, B_x, B_c_kv, B_w_q, B_w_uk, B_w_uv, B_w_o):
    def _A_ref(x, k_cache, v_cache, wq, wo):
        B, Sq, D = x.shape
        Hq, Dh = 32, 192
        Hkv = k_cache.shape[1]
        rep = Hq // Hkv
        q = (lambda x, *d: x.permute(*d))((x @ wq).reshape(B, Sq, Hq, Dh), 0, 2, 1, 3)
        k = (lambda x, n, axis: torch.repeat_interleave(x, n, dim=axis))(k_cache, rep, axis=1)
        v = (lambda x, n, axis: torch.repeat_interleave(x, n, dim=axis))(v_cache, rep, axis=1)
        scores = torch.einsum("bhqd,bhkd->bhqk", q, k) * (Dh ** -0.5)
        p = _softmax(scores, axis=-1)
        o = torch.einsum("bhqk,bhkd->bhqd", p, v)
        return (lambda x, *d: x.permute(*d))(o, 0, 2, 1, 3).reshape(B, Sq, Hq * Dh) @ wo

    def _B_ref(x, c_kv, w_q, w_uk, w_uv, w_o):
        B, Sq, M = x.shape[0], x.shape[1], x.shape[2]
        Sk = c_kv.shape[1]
        H, D = w_q.shape[1], w_q.shape[2]
        q = torch.einsum("bqm,mhd->bhqd", x, w_q)
        k = torch.einsum("bkc,chd->bhkd", c_kv, w_uk)
        v = torch.einsum("bkc,chd->bhkd", c_kv, w_uv)
        scores = torch.einsum("bhqd,bhkd->bhqk", q, k) * (D ** -0.5)
        off = Sk - Sq
        qi = torch.arange(Sq)[:, None]
        kj = torch.arange(Sk)[None, :]
        cmask = kj <= (qi + off)
        scores = torch.where(cmask[None, None], scores, -1e9)
        p = _softmax(scores, axis=-1)
        ctx = torch.einsum("bhqk,bhkd->bhqd", p, v)
        return torch.einsum("bhqd,hdm->bqm", ctx, w_o)

    x = A_x
    x = x + _A_ref(_layernorm(x, axis=-1), A_k_cache, A_v_cache, A_wq, A_wo)
    x = x + _B_ref(_layernorm(x, axis=-1), B_c_kv, B_w_q, B_w_uk, B_w_uv, B_w_o)
    x = x + _A_ref(_layernorm(x, axis=-1), A_k_cache, A_v_cache, A_wq, A_wo)
    x = x + _B_ref(_layernorm(x, axis=-1), B_c_kv, B_w_q, B_w_uk, B_w_uv, B_w_o)
    return x
```

```cpp
#include <hip/hip_runtime.h>
#include <hip/hip_bf16.h>
#include <stdint.h>

typedef unsigned short u16;
typedef unsigned v4u __attribute__((ext_vector_type(4)));

constexpr int DM = 640, H = 32, DH = 192, SK = 4096, DC = 768, HD = H * DH;

__device__ __forceinline__ float bf2f(u16 b) { return __builtin_bit_cast(float, (unsigned)b << 16); }
__device__ __forceinline__ float bflo(unsigned u) { return __builtin_bit_cast(float, u << 16); }
__device__ __forceinline__ float bfhi(unsigned u) { return __builtin_bit_cast(float, u & 0xffff0000u); }
__device__ __forceinline__ u16 f2bf(float f) { __hip_bfloat16 b = __float2bfloat16(f); return __builtin_bit_cast(u16, b); }
__device__ __forceinline__ float rbf(float f) { return bf2f(f2bf(f)); }

__device__ __forceinline__ float wave_sum(float v) {
  for (int o = 32; o > 0; o >>= 1) v += __shfl_xor(v, o);
  return v;
}
__device__ __forceinline__ float wave_max(float v) {
  for (int o = 32; o > 0; o >>= 1) v = fmaxf(v, __shfl_xor(v, o));
  return v;
}

__global__ void __launch_bounds__(256) k_qproj(const u16* x, const u16* w, float* q) {
  __shared__ float xn[DM];
  __shared__ float red[4][64];
  __shared__ float st[2];
  int tid = threadIdx.x, lane = tid & 63, wv = tid >> 6;
  if (wv == 0) {
    float s = 0;
    for (int i = lane; i < DM; i += 64) s += bf2f(x[i]);
    s = wave_sum(s) / DM;
    float v = 0;
    for (int i = lane; i < DM; i += 64) { float d = bf2f(x[i]) - s; v += d * d; }
    v = wave_sum(v) / DM;
    if (lane == 0) { st[0] = s; st[1] = rsqrtf(v + 1e-5f); }
  }
  __syncthreads();
  for (int i = tid; i < DM; i += 256) xn[i] = rbf((bf2f(x[i]) - st[0]) * st[1]);
  __syncthreads();
  int col = blockIdx.x * 64 + lane;
  float acc = 0;
  for (int r = wv * 160; r < wv * 160 + 160; ++r) acc += xn[r] * bf2f(w[(size_t)r * HD + col]);
  red[wv][lane] = acc;
  __syncthreads();
  if (wv == 0) q[col] = rbf(red[0][lane] + red[1][lane] + red[2][lane] + red[3][lane]);
}

constexpr int NSPL = 32, KPB = SK / NSPL;
__global__ void __launch_bounds__(256) k_attnA(const float* q, const u16* K, const u16* V, float* pm, float* pl, float* po) {
  __shared__ float qs[DH];
  __shared__ float sc[KPB];
  __shared__ float red[8][DH];
  __shared__ float mm[4];
  int s = blockIdx.x, h = blockIdx.y, tid = threadIdx.x, lane = tid & 63, wv = tid >> 6;
  if (tid < DH) qs[tid] = q[h * DH + tid];
  __syncthreads();
  const u16* Kb = K + ((size_t)h * SK + s * KPB) * DH;
  const u16* Vb = V + ((size_t)h * SK + s * KPB) * DH;
  int sub = tid & 7, kk = tid >> 3;
  for (int p = 0; p < KPB / 32; ++p) {
    int key = p * 32 + kk;
    float a = 0;
    for (int j = 0; j < 3; ++j) {
      int d0 = (j * 8 + sub) * 8;
      v4u u = *(const v4u*)(Kb + (size_t)key * DH + d0);
      for (int e = 0; e < 4; ++e) a += bflo(u[e]) * qs[d0 + 2 * e] + bfhi(u[e]) * qs[d0 + 2 * e + 1];
    }
    a += __shfl_xor(a, 1); a += __shfl_xor(a, 2); a += __shfl_xor(a, 4);
    if (sub == 0) sc[key] = a * 0.07216878364870322f;
  }
  __syncthreads();
  float m = -INFINITY;
  if (tid < KPB) m = sc[tid];
  m = wave_max(m);
  if (lane == 0) mm[wv] = m;
  __syncthreads();
  m = fmaxf(fmaxf(mm[0], mm[1]), fmaxf(mm[2], mm[3]));
  __syncthreads();
  float e = 0;
  if (tid < KPB) { e = __expf(sc[tid] - m); sc[tid] = e; }
  e = wave_sum(e);
  if (lane == 0) mm[wv] = e;
  __syncthreads();
  float l = mm[0] + mm[1] + mm[2] + mm[3];
  int g = tid >> 5, c = tid & 31;
  float acc[8] = {0, 0, 0, 0, 0, 0, 0, 0};
  if (c < 24) {
    for (int key = g; key < KPB; key += 8) {
      v4u u = *(const v4u*)(Vb + (size_t)key * DH + c * 8);
      float p = sc[key];
      for (int e2 = 0; e2 < 4; ++e2) { acc[2 * e2] += p * bflo(u[e2]); acc[2 * e2 + 1] += p * bfhi(u[e2]); }
    }
    for (int e2 = 0; e2 < 8; ++e2) red[g][c * 8 + e2] = acc[e2];
  }
  __syncthreads();
  if (tid < DH) {
    float o = 0;
    for (int i = 0; i < 8; ++i) o += red[i][tid];
    po[((size_t)h * NSPL + s) * DH + tid] = o;
  }
  if (tid == 0) { pm[h * NSPL + s] = m; pl[h * NSPL + s] = l; }
}

__global__ void k_combA(const float* pm, const float* pl, const float* po, float* o) {
  int h = blockIdx.x, tid = threadIdx.x;
  float M = -INFINITY;
  for (int s = 0; s < NSPL; ++s) M = fmaxf(M, pm[h * NSPL + s]);
  float L = 0, a = 0;
  for (int s = 0; s < NSPL; ++s) { float w = __expf(pm[h * NSPL + s] - M); L += w * pl[h * NSPL + s]; a += w * po[((size_t)h * NSPL + s) * DH + tid]; }
  o[h * DH + tid] = rbf(a / L);
}

__global__ void __launch_bounds__(320) k_oproj(const float* o, const u16* w, float* part) {
  int b = blockIdx.x, tid = threadIdx.x;
  float a0 = 0, a1 = 0;
  for (int r = b * 32; r < b * 32 + 32; ++r) {
    unsigned u = *(const unsigned*)(w + (size_t)r * DM + tid * 2);
    float v = o[r];
    a0 += v * bflo(u); a1 += v * bfhi(u);
  }
  part[b * DM + tid * 2] = a0; part[b * DM + tid * 2 + 1] = a1;
}
__global__ void __launch_bounds__(640) k_resid(const u16* x, const float* part, u16* xo) {
  int tid = threadIdx.x;
  float a = 0;
  for (int b = 0; b < 192; ++b) a += part[b * DM + tid];
  xo[tid] = f2bf(bf2f(x[tid]) + rbf(a));
}

__global__ void __launch_bounds__(256) k_qabs(const float* q, const u16* wuk, float* qa) {
  int tid = threadIdx.x;
  int row = blockIdx.x * 32 + (tid >> 3);
  int sub = tid & 7;
  int h = row & 31, c = row >> 5;
  float a = 0;
  for (int j = 0; j < 3; ++j) {
    int d0 = (j * 8 + sub) * 8;
    v4u u = *(const v4u*)(wuk + (size_t)row * DH + d0);
    for (int e = 0; e < 4; ++e) a += bflo(u[e]) * q[h * DH + d0 + 2 * e] + bfhi(u[e]) * q[h * DH + d0 + 2 * e + 1];
  }
  a += __shfl_xor(a, 1); a += __shfl_xor(a, 2); a += __shfl_xor(a, 4);
  if (sub == 0) qa[h * DC + c] = rbf(a);
}

constexpr int NKG = 64, KB = SK / NKG;
__global__ void __launch_bounds__(256) k_attnB(const float* qa, const u16* ckv, float* pm, float* pl, float* pc) {
  __shared__ float qs[8][DC];
  __shared__ float sc[8][KB];
  int kg = blockIdx.x, hg = blockIdx.y, tid = threadIdx.x, lane = tid & 63, wv = tid >> 6;
  for (int i = tid; i < 8 * DC; i += 256) qs[i / DC][i % DC] = qa[hg * 8 * DC + i];
  __syncthreads();
  const u16* Cb = ckv + (size_t)kg * KB * DC;
  for (int k = wv * 16; k < wv * 16 + 16; ++k) {
    float cv[12];
    for (int j = 0; j < 12; ++j) cv[j] = bf2f(Cb[(size_t)k * DC + j * 64 + lane]);
    for (int h = 0; h < 8; ++h) {
      float a = 0;
      for (int j = 0; j < 12; ++j) a += cv[j] * qs[h][j * 64 + lane];
      a = wave_sum(a);
      if (lane == 0) sc[h][k] = a * 0.07216878364870322f;
    }
  }
  __syncthreads();
  for (int h = wv * 2; h < wv * 2 + 2; ++h) {
    float v = sc[h][lane];
    float m = wave_max(v);
    float e = __expf(v - m);
    float l = wave_sum(e);
    sc[h][lane] = rbf(e);
    if (lane == 0) { pm[(hg * 8 + h) * NKG + kg] = m; pl[(hg * 8 + h) * NKG + kg] = l; }
  }
  __syncthreads();
  for (int c = tid; c < DC; c += 256) {
    float acc[8] = {0, 0, 0, 0, 0, 0, 0, 0};
    for (int k = 0; k < KB; ++k) {
      float v = bf2f(Cb[(size_t)k * DC + c]);
      for (int h = 0; h < 8; ++h) acc[h] += sc[h][k] * v;
    }
    for (int h = 0; h < 8; ++h) pc[((size_t)(hg * 8 + h) * NKG + kg) * DC + c] = acc[h];
  }
}

__global__ void __launch_bounds__(192) k_uv(const float* pm, const float* pl, const float* pc, const u16* wuv, float* pctx) {
  __shared__ float cc[96];
  int h = blockIdx.x, s = blockIdx.y, tid = threadIdx.x;
  float M = -INFINITY;
  for (int g = 0; g < NKG; ++g) M = fmaxf(M, pm[h * NKG + g]);
  if (tid < 96) {
    float L = 0, a = 0;
    for (int g = 0; g < NKG; ++g) { float w = __expf(pm[h * NKG + g] - M); L += w * pl[h * NKG + g]; a += w * pc[((size_t)h * NKG + g) * DC + s * 96 + tid]; }
    cc[tid] = rbf(a / L);
  }
  __syncthreads();
  float a = 0;
  for (int c = 0; c < 96; ++c) a += cc[c] * bf2f(wuv[((size_t)(s * 96 + c) * H + h) * DH + tid]);
  pctx[(h * 8 + s) * DH + tid] = a;
}
__global__ void k_ctxsum(const float* pctx, float* o) {
  int h = blockIdx.x, tid = threadIdx.x;
  float a = 0;
  for (int s = 0; s < 8; ++s) a += pctx[(h * 8 + s) * DH + tid];
  o[h * DH + tid] = rbf(a);
}

extern "C" void kernel_launch(void* const* d_in, const int* in_sizes, int n_in, void* d_out, int out_size, void* d_ws, size_t ws_size, hipStream_t stream) {
  const u16* Ax = (const u16*)d_in[0];
  const u16* K = (const u16*)d_in[1];
  const u16* V = (const u16*)d_in[2];
  const u16* wq = (const u16*)d_in[3];
  const u16* wo = (const u16*)d_in[4];
  const u16* ckv = (const u16*)d_in[6];
  const u16* bwq = (const u16*)d_in[7];
  const u16* wuk = (const u16*)d_in[8];
  const u16* wuv = (const u16*)d_in[9];
  const u16* bwo = (const u16*)d_in[10];
  float* ws = (float*)d_ws;
  float* q = ws;
  float* o = q + 8192;
  float* pm = o + 8192;
  float* pl = pm + 4096;
  float* qa = pl + 4096;
  float* part = qa + 32768;
  float* pctx = part + 131072;
  float* po = pctx + 65536;
  float* pc = po + 262144;
  u16* xs = (u16*)(pc + 2097152);
  const u16* xin = Ax;
  for (int L = 0; L < 4; ++L) {
    u16* xo = (L == 3) ? (u16*)d_out : xs + L * 1024;
    if ((L & 1) == 0) {
      k_qproj<<<96, 256, 0, stream>>>(xin, wq, q);
      k_attnA<<<dim3(NSPL, H), 256, 0, stream>>>(q, K, V, pm, pl, po);
      k_combA<<<H, DH, 0, stream>>>(pm, pl, po, o);
      k_oproj<<<192, 320, 0, stream>>>(o, wo, part);
    } else {
      k_qproj<<<96, 256, 0, stream>>>(xin, bwq, q);
      k_qabs<<<DC * H / 32, 256, 0, stream>>>(q, wuk, qa);
      k_attnB<<<dim3(NKG, 4), 256, 0, stream>>>(qa, ckv, pm, pl, pc);
      k_uv<<<dim3(H, 8), 192, 0, stream>>>(pm, pl, pc, wuv, pctx);
      k_ctxsum<<<H, DH, 0, stream>>>(pctx, o);
      k_oproj<<<192, 320, 0, stream>>>(o, bwo, part);
    }
    k_resid<<<1, 640, 0, stream>>>(xin, part, xo);
    xin = xo;
  }
}
```

```cpp
#include <hip/hip_runtime.h>

__global__ void __launch_bounds__(256, 1) decode_megakernel(const void* x, const void* k, const void* wq, const void* v, const void* wo,
    const void* ckv, const void* bq, const void* uk, const void* uv, const void* bo, void* out, void* ws) {
  __shared__ __attribute__((aligned(16))) unsigned char lds[163840];
  unsigned tid = threadIdx.x;
  asm volatile(
  "s_mov_b32 s86, %2\n"
  "s_load_dwordx2 s[12:13], %1, 0x8\n"
  "s_load_dwordx2 s[8:9], %1, 0x0\n"
  "s_load_dwordx2 s[20:21], %1, 0x10\n"
  "s_load_dwordx2 s[56:57], %1, 0x58\n"
  "v_lshrrev_b32 v1, 6, v0\n"
  "v_and_b32 v36, 7, v0\n"
  "v_readfirstlane_b32 s33, v1\n"
  "v_mul_u32_u24 v36, 384, v36\n"
  "v_and_b32 v37, 8, v0\n"
  "v_lshl_add_u32 v36, v37, 3, v36\n"
  "v_and_b32 v37, 0x30, v0\n"
  "v_add_u32 v223, v36, v37\n"
  "s_and_b32 s70, s86, 7\n"
  "s_lshl_b32 s70, s70, 7\n"
  "s_and_b32 s71, s86, 0x18\n"
  "s_lshl_b32 s71, s71, 2\n"
  "s_add_u32 s70, s70, s71\n"
  "s_lshr_b32 s71, s86, 5\n"
  "s_lshl2_add_u32 s70, s71, s70\n"
  "s_add_u32 s70, s70, s33\n"
  "s_mul_i32 s95, s70, 49152\n"
  "s_mov_b32 s14, 50331648\n"
  "s_mov_b32 s15, 0x20000\n"
  "s_waitcnt lgkmcnt(0)\n"
  "buffer_load_dwordx4 a[0:3], v223, s[12:15], s95 offen offset:0 nt\n"
  "buffer_load_dwordx4 a[8:11], v223, s[12:15], s95 offen offset:128 nt\n"
  "buffer_load_dwordx4 a[16:19], v223, s[12:15], s95 offen offset:256 nt\n"
  "s_mov_b32 s10, 1280\n"
  "s_mov_b32 s11, 0x20000\n"
  "s_mov_b32 s22, 7864320\n"
  "s_mov_b32 s23, 0x20000\n"
  "s_mov_b32 s58, 13631488\n"
  "s_mov_b32 s59, 0x20000\n"
  "v_and_b32 v0, 63, v0\n"
  "s_and_b32 s70, s86, 7\n"
  "s_lshr_b32 s71, s86, 3\n"
  "s_and_b32 s60, s71, 3\n"
  "s_lshr_b32 s35, s71, 2\n"
  "s_lshl_b32 s34, s70, 2\n"
  "s_add_u32 s34, s34, s60\n"
  "s_lshl_b32 s61, s70, 3\n"
  "s_mul_i32 s84, s33, 480\n"
  "s_mul_i32 s70, s34, 384\n"
  "s_mul_i32 s81, s35, 983040\n"
  "s_add_u32 s81, s81, s70\n"
  "s_mul_i32 s77, s35, 160\n"
  "s_add_u32 s70, s84, 0\n"
  "v_add_u32 v33, s70, v0\n"
  "v_mul_u32_u24 v34, 2731, v33\n"
  "v_lshrrev_b32 v34, 16, v34\n"
  "v_mul_u32_u24 v30, 24, v34\n"
  "v_sub_u32 v33, v33, v30\n"
  "v_and_b32 v30, 6, v34\n"
  "v_xor_b32 v33, v33, v30\n"
  "v_mul_u32_u24 v30, 12288, v34\n"
  "v_lshl_add_u32 v30, v33, 4, v30\n"
  "s_add_u32 s70, s84, 64\n"
  "v_add_u32 v33, s70, v0\n"
  "v_mul_u32_u24 v34, 2731, v33\n"
  "v_lshrrev_b32 v34, 16, v34\n"
  "v_mul_u32_u24 v31, 24, v34\n"
  "v_sub_u32 v33, v33, v31\n"
  "v_and_b32 v31, 6, v34\n"
  "v_xor_b32 v33, v33, v31\n"
  "v_mul_u32_u24 v31, 12288, v34\n"
  "v_lshl_add_u32 v31, v33, 4, v31\n"
  "s_add_u32 s70, s84, 128\n"
  "v_add_u32 v33, s70, v0\n"
  "v_mul_u32_u24 v34, 2731, v33\n"
  "v_lshrrev_b32 v34, 16, v34\n"
  "v_mul_u32_u24 v32, 24, v34\n"
  "v_sub_u32 v33, v33, v32\n"
  "v_and_b32 v32, 6, v34\n"
  "v_xor_b32 v33, v33, v32\n"
  "v_mul_u32_u24 v32, 12288, v34\n"
  "v_lshl_add_u32 v32, v33, 4, v32\n"
  "v_min_u32 v35, 39, v0\n"
  "v_lshlrev_b32 v35, 2, v35\n"
  "s_waitcnt lgkmcnt(0)\n"
  "s_load_dword s62, s[56:57], 0x0\n"
  "s_mov_b32 s70, 0x1234\n"
  "s_mov_b32 s70, 0x1234\n"
  "s_mov_b32 s70, 0x1234\n"
  "s_nop 0\n"
  "buffer_load_dword v38, v35, s[8:11], s77 offen\n"
  "s_lshl_b32 s70, s84, 4\n"
  "s_add_u32 m0, s70, 0\n"
  "s_add_u32 s71, s81, 0\n"
  "buffer_load_dwordx4 v30, s[20:23], s71 offen  lds\n"
  "s_lshl_b32 s70, s84, 4\n"
  "s_add_u32 m0, s70, 1024\n"
  "s_add_u32 s71, s81, 0\n"
  "buffer_load_dwordx4 v31, s[20:23], s71 offen  lds\n"
  "s_lshl_b32 s70, s84, 4\n"
  "s_add_u32 m0, s70, 2048\n"
  "s_add_u32 s71, s81, 0\n"
  "buffer_load_dwordx4 v32, s[20:23], s71 offen  lds\n"
  "s_lshl_b32 s70, s84, 4\n"
  "s_add_u32 m0, s70, 3072\n"
  "s_add_u32 s71, s81, 98304\n"
  "buffer_load_dwordx4 v30, s[20:23], s71 offen  lds\n"
  "s_lshl_b32 s70, s84, 4\n"
  "s_add_u32 m0, s70, 4096\n"
  "s_add_u32 s71, s81, 98304\n"
  "buffer_load_dwordx4 v31, s[20:23], s71 offen  lds\n"
  "s_lshl_b32 s70, s84, 4\n"
  "s_add_u32 m0, s70, 5120\n"
  "s_add_u32 s71, s81, 98304\n"
  "buffer_load_dwordx4 v32, s[20:23], s71 offen  lds\n"
  "s_lshl_b32 s70, s84, 4\n"
  "s_add_u32 m0, s70, 6144\n"
  "s_add_u32 s71, s81, 196608\n"
  "buffer_load_dwordx4 v30, s[20:23], s71 offen  lds\n"
  "s_lshl_b32 s70, s84, 4\n"
  "s_add_u32 m0, s70, 7168\n"
  "s_add_u32 s71, s81, 196608\n"
  "s_mov_b32 exec_lo, 0xffffffff\n"
  "s_mov_b32 exec_hi, 0x0\n"
  "buffer_load_dwordx4 v31, s[20:23], s71 offen  lds\n"
  "s_mov_b64 exec, -1\n"
  "s_add_u32 s71, s95, 3072\n"
  "buffer_load_dwordx4 a[48:51], v223, s[12:15], s71 offen offset:0 nt\n"
  "buffer_load_dwordx4 a[56:59], v223, s[12:15], s71 offen offset:128 nt\n"
  "buffer_load_dwordx4 a[64:67], v223, s[12:15], s71 offen offset:256 nt\n"
  "s_add_u32 s71, s95, 6144\n"
  "buffer_load_dwordx4 a[4:7], v223, s[12:15], s71 offen offset:0 nt\n"
  "buffer_load_dwordx4 a[12:15], v223, s[12:15], s71 offen offset:128 nt\n"
  "buffer_load_dwordx4 a[20:23], v223, s[12:15], s71 offen offset:256 nt\n"
  "s_add_u32 s71, s95, 9216\n"
  "buffer_load_dwordx4 a[52:55], v223, s[12:15], s71 offen offset:0 nt\n"
  "buffer_load_dwordx4 a[60:63], v223, s[12:15], s71 offen offset:128 nt\n"
  "buffer_load_dwordx4 a[68:71], v223, s[12:15], s71 offen offset:256 nt\n"
  "s_add_u32 s71, s95, 12288\n"
  "buffer_load_dwordx4 a[24:27], v223, s[12:15], s71 offen offset:0 nt\n"
  "buffer_load_dwordx4 a[32:35], v223, s[12:15], s71 offen offset:128 nt\n"
  "buffer_load_dwordx4 a[40:43], v223, s[12:15], s71 offen offset:256 nt\n"
  "s_add_u32 s71, s95, 15360\n"
  "buffer_load_dwordx4 a[72:75], v223, s[12:15], s71 offen offset:0 nt\n"
  "buffer_load_dwordx4 a[80:83], v223, s[12:15], s71 offen offset:128 nt\n"
  "buffer_load_dwordx4 a[88:91], v223, s[12:15], s71 offen offset:256 nt\n"
  "s_load_dwordx2 s[16:17], %1, 0x18\n"
  "s_load_dwordx2 s[24:25], %1, 0x20\n"
  "s_load_dwordx2 s[28:29], %1, 0x28\n"
  "s_load_dwordx2 s[36:37], %1, 0x30\n"
  "s_load_dwordx2 s[40:41], %1, 0x38\n"
  "s_load_dwordx2 s[44:45], %1, 0x40\n"
  "s_load_dwordx2 s[48:49], %1, 0x48\n"
  "s_load_dwordx2 s[52:53], %1, 0x50\n"
  "s_memrealtime s[96:97]\n"
  "s_mov_b32 s18, 50331648\n"
  "s_mov_b32 s19, 0x20000\n"
  "s_mov_b32 s26, 7864320\n"
  "s_mov_b32 s27, 0x20000\n"
  "s_mov_b32 s30, 6291456\n"
  "s_mov_b32 s31, 0x20000\n"
  "s_mov_b32 s38, 7864320\n"
  "s_mov_b32 s39, 0x20000\n"
  "s_mov_b32 s42, 9437184\n"
  "s_mov_b32 s43, 0x20000\n"
  "s_mov_b32 s46, 9437184\n"
  "s_mov_b32 s47, 0x20000\n"
  "s_mov_b32 s50, 7864320\n"
  "s_mov_b32 s51, 0x20000\n"
  "s_mov_b32 s54, 1280\n"
  "s_mov_b32 s55, 0x20000\n"
  "s_add_u32 s61, s61, s35\n"
  "s_lshl_b32 s83, s34, 3\n"
  "s_add_u32 s83, s83, s35\n"
  "s_mul_i32 s70, s34, 384\n"
  "s_lshl_b32 s71, s35, 2\n"
  "s_add_u32 s71, s71, s33\n"
  "s_mul_i32 s85, s71, 294912\n"
  "s_add_u32 s85, s85, s70\n"
  "s_mul_i32 s82, s34, 245760\n"
  "s_add_u32 s82, s82, s77\n"
  "s_lshl_b32 s71, s83, 2\n"
  "s_add_u32 s71, s71, s33\n"
  "s_mul_i32 s95, s71, 49152\n"
  "s_mov_b32 s78, 0x00ff00ff\n"
  "s_mov_b32 s79, 0x00ff00ff\n"
  "s_mov_b32 s80, 0x3dd53b95\n"
  "s_getreg_b32 s87, hwreg(HW_REG_XCC_ID, 0, 3)\n"
  "s_mov_b32 s63, 0\n"
  "v_lshlrev_b32 v40, 3, v0\n"
  "v_min_u32 v40, 376, v40\n"
  "v_add_u32 v40, 159744, v40\n"
  "v_mov_b32 v42, 0\n"
  "v_mov_b32 v43, 0\n"
  "v_add_u32 v41, 160128, v35\n"
  "v_mov_b32 v44, 0x3f803f80\n"
  "v_add_u32 v35, 160320, v35\n"
  "s_waitcnt lgkmcnt(0)\n"
  "s_or_b32 s62, s62, 0x40004003\n"
  "s_add_u32 s62, s62, 1\n"
  "s_and_b32 s62, s62, 0xbfffbfff\n"
  "ds_write_b64 v40, v[42:43]\n"
  "ds_write_b32 v41, v44\n"
  "v_lshrrev_b32 v3, 2, v0\n"
  "v_mul_u32_u24 v2, 384, v3\n"
  "v_and_b32 v3, 6, v3\n"
  "v_lshl_add_u32 v2, v3, 4, v2\n"
  "v_and_b32 v3, 3, v0\n"
  "v_lshl_add_u32 v2, v3, 3, v2\n"
  "s_mul_i32 s70, s33, 3\n"
  "s_add_u32 s70, s70, 0\n"
  "s_and_b32 s71, s70, 3\n"
  "s_lshl_b32 s71, s71, 5\n"
  "s_lshr_b32 s70, s70, 2\n"
  "s_lshl_b32 s70, s70, 7\n"
  "v_xor_b32 v46, s71, v2\n"
  "v_add_u32 v46, s70, v46\n"
  "s_mul_i32 s70, s33, 3\n"
  "s_add_u32 s70, s70, 1\n"
  "s_and_b32 s71, s70, 3\n"
  "s_lshl_b32 s71, s71, 5\n"
  "s_lshr_b32 s70, s70, 2\n"
  "s_lshl_b32 s70, s70, 7\n"
  "v_xor_b32 v47, s71, v2\n"
  "v_add_u32 v47, s70, v47\n"
  "s_mul_i32 s70, s33, 3\n"
  "s_add_u32 s70, s70, 2\n"
  "s_and_b32 s71, s70, 3\n"
  "s_lshl_b32 s71, s71, 5\n"
  "s_lshr_b32 s70, s70, 2\n"
  "s_lshl_b32 s70, s70, 7\n"
  "v_xor_b32 v48, s71, v2\n"
  "v_add_u32 v48, s70, v48\n"
  "s_mov_b32 s66, 0x02100210\n"
  "s_mov_b32 s67, 0x02100210\n"
  "v_lshrrev_b32 v2, 4, v0\n"
  "v_lshlrev_b32 v2, 3, v2\n"
  "v_add_u32 v22, 160320, v2\n"
  "v_add_u32 v2, 160128, v2\n"
  "v_cndmask_b32_e64 v22, v22, v2, s[66:67]\n"
  "s_lshl_b32 s70, s34, 5\n"
  "s_lshl_b32 s71, s33, 3\n"
  "s_add_u32 s70, s70, s71\n"
  "s_add_u32 s70, s70, s35\n"
  "s_lshl_b32 s71, s70, 4\n"
  "s_lshl_b32 s70, s70, 8\n"
  "s_add_u32 s71, s71, 589824\n"
  "v_lshlrev_b32 v54, 4, v0\n"
  "v_add_u32 v54, s70, v54\n"
  "v_add_u32 v2, 327424, v54\n"
  "v_add_u32 v54, 65536, v54\n"
  "s_mov_b32 s66, 0xffff0000\n"
  "s_mov_b32 s67, 0\n"
  "v_cndmask_b32_e64 v54, v54, v2, s[66:67]\n"
  "v_mov_b32 v2, s71\n"
  "s_mov_b32 s66, 0\n"
  "s_mov_b32 s67, -1\n"
  "v_cndmask_b32_e64 v54, v54, v2, s[66:67]\n"
  "LPAD:\n"
  "s_waitcnt vmcnt(15)\n"
  "ds_write_b32 v35, v38\n"
  "s_waitcnt lgkmcnt(0)\n"
  "s_barrier\n"
  "ds_read2_b64 v[6:9], v22 offset0:0 offset1:4\n"
  "ds_read2_b64 v[10:13], v22 offset0:8 offset1:12\n"
  "ds_read_b64 v[4:5], v22 offset:128\n"
  "v_mov_b32 v2, 0\n"
  "v_mov_b32 v3, 0\n"
  "s_mov_b32 s66, 0\n"
  "s_mov_b32 s67, -1\n"
  "ds_read_b64_tr_b16 v[16:17], v46 offset:0\n"
  "ds_read_b64_tr_b16 v[18:19], v46 offset:6144\n"
  "ds_read_b64_tr_b16 v[20:21], v46 offset:12288\n"
  "ds_read_b64_tr_b16 v[22:23], v46 offset:18432\n"
  "ds_read_b64_tr_b16 v[24:25], v46 offset:24576\n"
  "ds_read_b64_tr_b16 v[26:27], v47 offset:0\n"
  "ds_read_b64_tr_b16 v[28:29], v47 offset:6144\n"
  "ds_read_b64_tr_b16 v[30:31], v47 offset:12288\n"
  "ds_read_b64_tr_b16 v[32:33], v47 offset:18432\n"
  "ds_read_b64_tr_b16 v[34:35], v47 offset:24576\n"
  "s_waitcnt lgkmcnt(10)\n"
  "v_mfma_f32_16x16x32_bf16 v[50:53], v[6:9], v[6:9], 0\n"
  "v_mfma_f32_16x16x32_bf16 v[50:53], v[10:13], v[10:13], v[50:53]\n"
  "v_mfma_f32_16x16x32_bf16 v[50:53], v[2:5], v[2:5], v[50:53]\n"
  "ds_read_b64_tr_b16 v[36:37], v48 offset:0\n"
  "ds_read_b64_tr_b16 v[38:39], v48 offset:6144\n"
  "ds_read_b64_tr_b16 v[40:41], v48 offset:12288\n"
  "ds_read_b64_tr_b16 v[42:43], v48 offset:18432\n"
  "ds_read_b64_tr_b16 v[44:45], v48 offset:24576\n"
  "s_waitcnt lgkmcnt(10)\n"
  "v_mfma_f32_16x16x32_bf16 v[46:49], v[6:9], v[16:19], 0\n"
  "v_mfma_f32_16x16x32_bf16 v[46:49], v[10:13], v[20:23], v[46:49]\n"
  "v_mfma_f32_16x16x32_bf16 v[46:49], v[2:5], v[22:25], v[46:49]\n"
  "s_waitcnt lgkmcnt(5)\n"
  "v_mfma_f32_16x16x32_bf16 v[16:19], v[6:9], v[26:29], 0\n"
  "v_mfma_f32_16x16x32_bf16 v[16:19], v[10:13], v[30:33], v[16:19]\n"
  "v_mfma_f32_16x16x32_bf16 v[16:19], v[2:5], v[32:35], v[16:19]\n"
  "s_waitcnt lgkmcnt(0)\n"
  "v_mfma_f32_16x16x32_bf16 v[22:25], v[6:9], v[36:39], 0\n"
  "v_mfma_f32_16x16x32_bf16 v[22:25], v[10:13], v[40:43], v[22:25]\n"
  "v_mfma_f32_16x16x32_bf16 v[22:25], v[2:5], v[42:45], v[22:25]\n"
  "v_cndmask_b32_e64 v20, v46, v50, s[66:67]\n"
  "s_nop 2\n"
  "v_cndmask_b32_e64 v21, v16, v51, s[66:67]\n"
  "s_nop 2\n"
  "v_mov_b32 v23, s62\n"
  "s_mov_b32 exec_lo, 0xffffffff\n"
  "s_mov_b32 exec_hi, 0x1\n"
  "buffer_store_dwordx4 v[20:23], v54, s[56:59], 0 offen sc1\n"
  "s_mov_b64 exec, -1\n"
  "s_add_u32 s71, s95, 18432\n"
  "buffer_load_dwordx4 a[28:31], v223, s[12:15], s71 offen offset:0 nt\n"
  "buffer_load_dwordx4 a[36:39], v223, s[12:15], s71 offen offset:128 nt\n"
  "buffer_load_dwordx4 a[44:47], v223, s[12:15], s71 offen offset:256 nt\n"
  "s_add_u32 s71, s95, 21504\n"
  "buffer_load_dwordx4 a[76:79], v223, s[12:15], s71 offen offset:0 nt\n"
  "buffer_load_dwordx4 a[84:87], v223, s[12:15], s71 offen offset:128 nt\n"
  "buffer_load_dwordx4 a[92:95], v223, s[12:15], s71 offen offset:256 nt\n"
  "s_add_u32 s71, s95, 24576\n"
  "buffer_load_dwordx4 a[96:99], v223, s[12:15], s71 offen offset:0 nt\n"
  "buffer_load_dwordx4 a[104:107], v223, s[12:15], s71 offen offset:128 nt\n"
  "buffer_load_dwordx4 a[112:115], v223, s[12:15], s71 offen offset:256 nt\n"
  "s_add_u32 s71, s95, 27648\n"
  "buffer_load_dwordx4 a[144:147], v223, s[12:15], s71 offen offset:0 nt\n"
  "buffer_load_dwordx4 a[152:155], v223, s[12:15], s71 offen offset:128 nt\n"
  "buffer_load_dwordx4 a[160:163], v223, s[12:15], s71 offen offset:256 nt\n"
  "s_add_u32 s71, s95, 30720\n"
  "buffer_load_dwordx4 a[100:103], v223, s[12:15], s71 offen offset:0 nt\n"
  "buffer_load_dwordx4 a[108:111], v223, s[12:15], s71 offen offset:128 nt\n"
  "buffer_load_dwordx4 a[116:119], v223, s[12:15], s71 offen offset:256 nt\n"
  "s_add_u32 s71, s95, 33792\n"
  "buffer_load_dwordx4 a[148:151], v223, s[12:15], s71 offen offset:0 nt\n"
  "buffer_load_dwordx4 a[156:159], v223, s[12:15], s71 offen offset:128 nt\n"
  "buffer_load_dwordx4 a[164:167], v223, s[12:15], s71 offen offset:256 nt\n"
  "v_mov_b32 v228, -1\n"
  "v_mov_b32 v229, -1\n"
  "v_mov_b32 v230, -1\n"
  "v_mov_b32 v231, -1\n"
  "v_and_b32 v225, 1, v0\n"
  "v_lshlrev_b32 v225, 10, v225\n"
  "v_and_b32 v226, 8, v0\n"
  "v_lshl_add_u32 v225, v226, 8, v225\n"
  "v_and_b32 v226, 6, v0\n"
  "v_lshl_add_u32 v225, v226, 1, v225\n"
  "s_lshr_b32 s70, s34, 3\n"
  "s_lshl_b32 s70, s70, 2\n"
  "s_lshr_b32 s71, s35, 1\n"
  "s_add_u32 s70, s70, s71\n"
  "s_mul_i32 s72, s70, 3072\n"
  "s_and_b32 s70, s34, 4\n"
  "s_add_u32 s70, s70, s33\n"
  "s_lshl_b32 s70, s70, 1\n"
  "s_and_b32 s71, s35, 1\n"
  "s_add_u32 s70, s70, s71\n"
  "s_lshl_b32 s70, s70, 2\n"
  "s_and_b32 s71, s34, 3\n"
  "s_add_u32 s70, s70, s71\n"
  "s_lshl_b32 s70, s70, 4\n"
  "s_add_u32 s70, s70, 720896\n"
  "s_add_u32 s72, s72, s70\n"
  "s_mov_b32 exec_lo, 0x55ff\n"
  "s_mov_b32 exec_hi, 0x0\n"
  "buffer_store_dword v228, v225, s[56:59], s72 offen sc1\n"
  "s_mov_b64 exec, -1\n"
  "v_and_b32 v225, 1, v0\n"
  "v_lshlrev_b32 v225, 10, v225\n"
  "v_and_b32 v226, 8, v0\n"
  "v_lshl_add_u32 v225, v226, 8, v225\n"
  "v_and_b32 v226, 6, v0\n"
  "v_lshl_add_u32 v225, v226, 1, v225\n"
  "s_lshr_b32 s70, s34, 3\n"
  "s_lshl_b32 s70, s70, 2\n"
  "s_lshr_b32 s71, s35, 1\n"
  "s_add_u32 s70, s70, s71\n"
  "s_mul_i32 s72, s70, 3072\n"
  "s_and_b32 s70, s34, 4\n"
  "s_add_u32 s70, s70, s33\n"
  "s_lshl_b32 s70, s70, 1\n"
  "s_and_b32 s71, s35, 1\n"
  "s_add_u32 s70, s70, s71\n"
  "s_lshl_b32 s70, s70, 2\n"
  "s_and_b32 s71, s34, 3\n"
  "s_add_u32 s70, s70, s71\n"
  "s_lshl_b32 s70, s70, 4\n"
  "s_add_u32 s70, s70, 786432\n"
  "s_add_u32 s72, s72, s70\n"
  "s_mov_b32 exec_lo, 0x55ff\n"
  "s_mov_b32 exec_hi, 0x0\n"
  "buffer_store_dword v228, v225, s[56:59], s72 offen sc1\n"
  "s_mov_b64 exec, -1\n"
  "v_add_u32 v34, 0, v0\n"
  "v_mul_u32_u24 v35, 2731, v34\n"
  "v_lshrrev_b32 v35, 16, v35\n"
  "v_mul_u32_u24 v220, 24, v35\n"
  "v_sub_u32 v34, v34, v220\n"
  "v_and_b32 v220, 6, v35\n"
  "v_xor_b32 v34, v34, v220\n"
  "v_mul_u32_u24 v220, 384, v35\n"
  "v_lshl_add_u32 v220, v34, 4, v220\n"
  "v_add_u32 v34, 64, v0\n"
  "v_mul_u32_u24 v35, 2731, v34\n"
  "v_lshrrev_b32 v35, 16, v35\n"
  "v_mul_u32_u24 v221, 24, v35\n"
  "v_sub_u32 v34, v34, v221\n"
  "v_and_b32 v221, 6, v35\n"
  "v_xor_b32 v34, v34, v221\n"
  "v_mul_u32_u24 v221, 384, v35\n"
  "v_lshl_add_u32 v221, v34, 4, v221\n"
  "v_add_u32 v34, 128, v0\n"
  "v_mul_u32_u24 v35, 2731, v34\n"
  "v_lshrrev_b32 v35, 16, v35\n"
  "v_mul_u32_u24 v222, 24, v35\n"
  "v_sub_u32 v34, v34, v222\n"
  "v_and_b32 v222, 6, v35\n"
  "v_xor_b32 v34, v34, v222\n"
  "v_mul_u32_u24 v222, 384, v35\n"
  "v_lshl_add_u32 v222, v34, 4, v222\n"
  "v_lshlrev_b32 v26, 4, v0\n"
  "v_lshrrev_b32 v27, 1, v0\n"
  "v_and_b32 v27, 7, v27\n"
  "v_lshlrev_b32 v27, 4, v27\n"
  "s_lshl_b32 s70, s34, 2\n"
  "s_add_u32 s70, s70, s33\n"
  "s_lshl_b32 s71, s70, 7\n"
  "s_lshl_b32 s70, s70, 11\n"
  "s_add_u32 s88, s71, 589824\n"
  "s_add_u32 s89, s70, 65536\n"
  "s_add_u32 s90, s70, 327680\n"
  "v_lshrrev_b32 v28, 4, v0\n"
  "v_lshlrev_b32 v28, 1, v28\n"
  "v_mov_b32 v29, 0x98\n"
  "v_lshrrev_b32 v28, v28, v29\n"
  "v_and_b32 v28, 3, v28\n"
  "v_and_b32 v29, 15, v0\n"
  "v_lshl_add_u32 v28, v28, 4, v29\n"
  "s_mul_i32 s70, s33, 48\n"
  "v_add_u32 v28, s70, v28\n"
  "v_lshlrev_b32 v29, 2, v28\n"
  "v_add_u32 v29, 160896, v29\n"
  "v_and_b32 v30, 0xe1, v28\n"
  "v_and_b32 v6, 0x1c, v28\n"
  "v_lshl_add_u32 v30, v30, 1, v6\n"
  "v_and_b32 v28, 2, v28\n"
  "v_lshl_add_u32 v28, v28, 4, v30\n"
  "v_add_u32 v28, 160512, v28\n"
  "v_and_b32 v6, 3, v0\n"
  "v_and_b32 v7, 16, v0\n"
  "v_lshl_add_u32 v6, v6, 5, v7\n"
  "v_add_u32 v6, 160512, v6\n"
  "v_mov_b32 v8, 159744\n"
  "v_and_b32 v31, 1, v0\n"
  "v_mul_u32_u24 v31, 0xaaaa, v31\n"
  "v_add_u32 v31, 0x4444, v31\n"
  "s_mov_b32 s66, 0xf000f\n"
  "s_mov_b32 s67, 0xf000f0\n"
  "v_cndmask_b32_e64 v48, v8, v6, s[66:67]\n"
  "s_mov_b32 s66, 0xf000f00\n"
  "s_mov_b32 s67, 0xf000f000\n"
  "v_cndmask_b32_e64 v49, v8, v6, s[66:67]\n"
  "v_mov_b32 v32, 0\n"
  "v_mov_b32 v33, 0\n"
  "v_mov_b32 v34, 0\n"
  "v_mov_b32 v35, 0\n"
  "v_mov_b32 v36, 0\n"
  "v_mov_b32 v37, 0\n"
  "v_mov_b32 v38, 0\n"
  "v_mov_b32 v39, 0\n"
  "v_mov_b32 v40, 0\n"
  "v_mov_b32 v41, 0\n"
  "v_mov_b32 v42, 0\n"
  "v_mov_b32 v43, 0\n"
  "v_mov_b32 v44, 0\n"
  "v_mov_b32 v45, 0\n"
  "v_mov_b32 v46, 0\n"
  "v_mov_b32 v47, 0\n"
  "s_lshl_b32 s70, s33, 8\n"
  "s_add_u32 s70, s70, 162560\n"
  "v_and_b32 v6, 0x30, v0\n"
  "v_add_u32 v6, s70, v6\n"
  "s_mov_b32 s66, 0xf000f\n"
  "s_mov_b32 s67, 0xf000f\n"
  "v_cndmask_b32_e64 v51, v8, v6, s[66:67]\n"
  "s_mov_b32 s66, 0xf000f0\n"
  "s_mov_b32 s67, 0xf000f0\n"
  "v_cndmask_b32_e64 v52, v8, v6, s[66:67]\n"
  "s_mov_b32 s66, 0xf000f00\n"
  "s_mov_b32 s67, 0xf000f00\n"
  "v_cndmask_b32_e64 v53, v8, v6, s[66:67]\n"
  "s_mov_b32 s66, 0xf000f000\n"
  "s_mov_b32 s67, 0xf000f000\n"
  "v_cndmask_b32_e64 v54, v8, v6, s[66:67]\n"
  "v_and_b32 v50, 0x23, v0\n"
  "v_and_b32 v6, 12, v0\n"
  "v_lshl_add_u32 v50, v6, 1, v50\n"
  "v_and_b32 v6, 16, v0\n"
  "v_lshrrev_b32 v6, 2, v6\n"
  "v_add_u32 v50, v50, v6\n"
  "v_lshlrev_b32 v50, 1, v50\n"
  "v_add_u32 v50, s70, v50\n"
  "LCA1:\n"
  "buffer_load_dwordx4 v[6:9], v27, s[56:59], s88 offen sc1\n"
  "buffer_load_dwordx4 v[18:21], v26, s[56:59], s90 offen sc1\n"
  "buffer_load_dwordx4 v[22:25], v26, s[56:59], s90 offen offset:1024 sc1\n"
  "buffer_load_dwordx4 v[10:13], v26, s[56:59], s89 offen sc1\n"
  "buffer_load_dwordx4 v[14:17], v26, s[56:59], s89 offen offset:1024 sc1\n"
  "s_add_u32 s71, s95, 36864\n"
  "buffer_load_dwordx4 a[120:123], v223, s[12:15], s71 offen offset:0 nt\n"
  "buffer_load_dwordx4 a[128:131], v223, s[12:15], s71 offen offset:128 nt\n"
  "buffer_load_dwordx4 a[136:139], v223, s[12:15], s71 offen offset:256 nt\n"
  "s_add_u32 s71, s95, 39936\n"
  "buffer_load_dwordx4 a[168:171], v223, s[12:15], s71 offen offset:0 nt\n"
  "buffer_load_dwordx4 a[176:179], v223, s[12:15], s71 offen offset:128 nt\n"
  "buffer_load_dwordx4 a[184:187], v223, s[12:15], s71 offen offset:256 nt\n"
  "s_add_u32 s71, s95, 43008\n"
  "buffer_load_dwordx4 a[124:127], v223, s[12:15], s71 offen offset:0 nt\n"
  "buffer_load_dwordx4 a[132:135], v223, s[12:15], s71 offen offset:128 nt\n"
  "buffer_load_dwordx4 a[140:143], v223, s[12:15], s71 offen offset:256 nt\n"
  "s_add_u32 s71, s95, 46080\n"
  "buffer_load_dwordx4 a[172:175], v223, s[12:15], s71 offen offset:0 nt\n"
  "buffer_load_dwordx4 a[180:183], v223, s[12:15], s71 offen offset:128 nt\n"
  "buffer_load_dwordx4 a[188:191], v223, s[12:15], s71 offen offset:256 nt\n"
  "s_add_u32 s72, s95, 0\n"
  "buffer_load_dwordx4 v[56:59], v220, s[16:19], s72 offen nt\n"
  "buffer_load_dwordx4 v[64:67], v221, s[16:19], s72 offen nt\n"
  "buffer_load_dwordx4 v[72:75], v222, s[16:19], s72 offen nt\n"
  "s_add_u32 s72, s95, 3072\n"
  "buffer_load_dwordx4 v[80:83], v220, s[16:19], s72 offen nt\n"
  "buffer_load_dwordx4 v[88:91], v221, s[16:19], s72 offen nt\n"
  "buffer_load_dwordx4 v[96:99], v222, s[16:19], s72 offen nt\n"
  "s_add_u32 s72, s95, 6144\n"
  "buffer_load_dwordx4 v[104:107], v220, s[16:19], s72 offen nt\n"
  "buffer_load_dwordx4 v[112:115], v221, s[16:19], s72 offen nt\n"
  "buffer_load_dwordx4 v[120:123], v222, s[16:19], s72 offen nt\n"
  "s_add_u32 s72, s95, 9216\n"
  "buffer_load_dwordx4 v[128:131], v220, s[16:19], s72 offen nt\n"
  "buffer_load_dwordx4 v[136:139], v221, s[16:19], s72 offen nt\n"
  "buffer_load_dwordx4 v[144:147], v222, s[16:19], s72 offen nt\n"
  "s_add_u32 s72, s95, 12288\n"
  "buffer_load_dwordx4 v[60:63], v220, s[16:19], s72 offen nt\n"
  "buffer_load_dwordx4 v[68:71], v221, s[16:19], s72 offen nt\n"
  "LPA1_1:\n"
  "s_waitcnt vmcnt(30)\n"
  "v_cmp_ne_u32_e64 s[64:65], s62, v9\n"
  "v_add_f32_dpp v6, v6, v6 quad_perm:[2,3,0,1] row_mask:0xf bank_mask:0xf\n"
  "v_add_f32_dpp v7, v7, v7 quad_perm:[2,3,0,1] row_mask:0xf bank_mask:0xf\n"
  "s_nop 0\n"
  "v_add_f32_dpp v6, v6, v6 row_ror:4 row_mask:0xf bank_mask:0xf\n"
  "v_add_f32_dpp v7, v7, v7 row_ror:4 row_mask:0xf bank_mask:0xf\n"
  "s_nop 0\n"
  "v_add_f32_dpp v6, v6, v6 row_ror:8 row_mask:0xf bank_mask:0xf\n"
  "v_add_f32_dpp v7, v7, v7 row_ror:8 row_mask:0xf bank_mask:0xf\n"
  "v_mul_f32 v7, 0x3acccccd, v7\n"
  "v_mul_f32 v8, v7, v7\n"
  "v_mov_b32 v30, 0x3acccccd\n"
  "v_fma_f32 v6, v6, v30, -v8\n"
  "v_max_f32 v6, 0, v6\n"
  "v_add_f32 v6, 0x3727c5ac, v6\n"
  "v_rsq_f32 v6, v6\n"
  "s_waitcnt vmcnt(28)\n"
  "v_cmp_ne_u32 vcc, s62, v21\n"
  "s_nop 0\n"
  "s_or_b64 s[64:65], s[64:65], vcc\n"
  "v_cmp_ne_u32 vcc, s62, v25\n"
  "s_nop 0\n"
  "s_or_b64 s[64:65], s[64:65], vcc\n"
  "v_add_f32 v18, v18, v22\n"
  "v_add_f32 v19, v19, v23\n"
  "v_add_f32 v20, v20, v24\n"
  "v_mov_b32 v30, v20\n"
  "v_permlane32_swap_b32 v18, v19\n"
  "s_nop 0\n"
  "v_permlane32_swap_b32 v20, v30\n"
  "v_add_f32 v18, v18, v19\n"
  "v_add_f32 v20, v20, v30\n"
  "s_nop 1\n"
  "v_permlane16_swap_b32 v18, v20\n"
  "v_add_f32 v18, v18, v20\n"
  "v_mul_f32 v7, v7, v18\n"
  "s_waitcnt vmcnt(27)\n"
  "v_cmp_ne_u32 vcc, s62, v13\n"
  "s_nop 0\n"
  "s_or_b64 s[64:65], s[64:65], vcc\n"
  "s_waitcnt vmcnt(26)\n"
  "v_cmp_ne_u32 vcc, s62, v17\n"
  "s_nop 0\n"
  "s_or_b64 s[64:65], s[64:65], vcc\n"
  "s_cbranch_scc1 LSA1_2\n"
  "v_add_f32 v10, v10, v14\n"
  "v_add_f32 v11, v11, v15\n"
  "v_add_f32 v12, v12, v16\n"
  "v_mov_b32 v30, v12\n"
  "v_permlane32_swap_b32 v10, v11\n"
  "s_nop 0\n"
  "v_permlane32_swap_b32 v12, v30\n"
  "v_add_f32 v10, v10, v11\n"
  "v_add_f32 v12, v12, v30\n"
  "s_nop 1\n"
  "v_permlane16_swap_b32 v10, v12\n"
  "v_add_f32 v10, v10, v12\n"
  "v_sub_f32 v10, v10, v7\n"
  "v_mul_f32 v10, v10, v6\n"
  "v_cvt_pk_bf16_f32 v10, v10, v10\n"
  "ds_write_b16 v28, v10\n"
  "ds_write_b32 v29, v18\n"
  "s_waitcnt lgkmcnt(0)\n"
  "s_barrier\n"
  "LCK:\n"
  "s_waitcnt vmcnt(14)\n"
  "ds_read_b128 v[6:9], v48 offset:0\n"
  "ds_read_b128 v[10:13], v48 offset:128\n"
  "ds_read_b128 v[14:17], v48 offset:256\n"
  "ds_read_b128 v[18:21], v49 offset:0\n"
  "ds_read_b128 v[22:25], v49 offset:128\n"
  "ds_read_b128 v[26:29], v49 offset:256\n"
  "s_waitcnt lgkmcnt(5)\n"
  "v_smfmac_f32_16x16x64_bf16 v[32:35], v[6:9], a[0:7], v31\n"
  "s_waitcnt lgkmcnt(4)\n"
  "v_smfmac_f32_16x16x64_bf16 v[32:35], v[10:13], a[8:15], v31\n"
  "s_waitcnt lgkmcnt(3)\n"
  "v_smfmac_f32_16x16x64_bf16 v[32:35], v[14:17], a[16:23], v31\n"
  "s_waitcnt lgkmcnt(2)\n"
  "v_smfmac_f32_16x16x64_bf16 v[32:35], v[18:21], a[24:31], v31\n"
  "s_waitcnt lgkmcnt(1)\n"
  "v_smfmac_f32_16x16x64_bf16 v[32:35], v[22:25], a[32:39], v31\n"
  "s_waitcnt lgkmcnt(0)\n"
  "v_smfmac_f32_16x16x64_bf16 v[32:35], v[26:29], a[40:47], v31\n"
  "v_smfmac_f32_16x16x64_bf16 v[36:39], v[6:9], a[48:55], v31\n"
  "v_smfmac_f32_16x16x64_bf16 v[36:39], v[10:13], a[56:63], v31\n"
  "v_smfmac_f32_16x16x64_bf16 v[36:39], v[14:17], a[64:71], v31\n"
  "v_smfmac_f32_16x16x64_bf16 v[36:39], v[18:21], a[72:79], v31\n"
  "v_smfmac_f32_16x16x64_bf16 v[36:39], v[22:25], a[80:87], v31\n"
  "v_smfmac_f32_16x16x64_bf16 v[36:39], v[26:29], a[88:95], v31\n"
  "v_smfmac_f32_16x16x64_bf16 v[40:43], v[6:9], a[96:103], v31\n"
  "v_smfmac_f32_16x16x64_bf16 v[40:43], v[10:13], a[104:111], v31\n"
  "v_smfmac_f32_16x16x64_bf16 v[40:43], v[14:17], a[112:119], v31\n"
  "v_smfmac_f32_16x16x64_bf16 v[40:43], v[18:21], a[120:127], v31\n"
  "v_add_f32 v32, v32, v33\n"
  "v_add_f32 v34, v34, v35\n"
  "v_smfmac_f32_16x16x64_bf16 v[40:43], v[22:25], a[128:135], v31\n"
  "s_nop 0\n"
  "v_add_f32 v36, v36, v37\n"
  "v_add_f32 v38, v38, v39\n"
  "v_smfmac_f32_16x16x64_bf16 v[40:43], v[26:29], a[136:143], v31\n"
  "v_cndmask_b32_e64 v36, v34, v36, s[78:79]\n"
  "v_cndmask_b32_e64 v32, v38, v32, s[78:79]\n"
  "v_smfmac_f32_16x16x64_bf16 v[44:47], v[6:9], a[144:151], v31\n"
  "s_nop 0\n"
  "v_add_f32_dpp v32, v36, v32 row_ror:8 row_mask:0xf bank_mask:0xf\n"
  "v_smfmac_f32_16x16x64_bf16 v[44:47], v[10:13], a[152:159], v31\n"
  "v_smfmac_f32_16x16x64_bf16 v[44:47], v[14:17], a[160:167], v31\n"
  "v_smfmac_f32_16x16x64_bf16 v[44:47], v[18:21], a[168:175], v31\n"
  "v_add_f32 v40, v40, v41\n"
  "v_add_f32 v42, v42, v43\n"
  "v_smfmac_f32_16x16x64_bf16 v[44:47], v[22:25], a[176:183], v31\n"
  "v_smfmac_f32_16x16x64_bf16 v[44:47], v[26:29], a[184:191], v31\n"
  "s_nop 7\n"
  "v_add_f32 v44, v44, v45\n"
  "v_add_f32 v46, v46, v47\n"
  "v_cndmask_b32_e64 v44, v42, v44, s[78:79]\n"
  "v_cndmask_b32_e64 v40, v46, v40, s[78:79]\n"
  "s_nop 1\n"
  "v_add_f32_dpp v40, v44, v40 row_ror:8 row_mask:0xf bank_mask:0xf\n"
  "v_max_f32 v33, v32, v40\n"
  "s_nop 1\n"
  "v_max_f32_dpp v33, v33, v33 quad_perm:[1,0,3,2] row_mask:0xf bank_mask:0xf\n"
  "s_nop 1\n"
  "v_max_f32_dpp v33, v33, v33 quad_perm:[2,3,0,1] row_mask:0xf bank_mask:0xf\n"
  "s_nop 1\n"
  "v_max_f32_dpp v33, v33, v33 row_ror:4 row_mask:0xf bank_mask:0xf\n"
  "s_nop 1\n"
  "v_max_f32_dpp v33, v33, v33 row_ror:8 row_mask:0xf bank_mask:0xf\n"
  "v_mov_b32 v34, v33\n"
  "s_nop 1\n"
  "v_permlane16_swap_b32 v33, v34\n"
  "v_max_f32 v33, v33, v34\n"
  "v_mov_b32 v34, v33\n"
  "s_nop 1\n"
  "v_permlane32_swap_b32 v33, v34\n"
  "v_max_f32 v33, v33, v34\n"
  "v_mul_f32 v39, s80, v33\n"
  "v_fma_f32 v32, v32, s80, -v39\n"
  "v_fma_f32 v40, v40, s80, -v39\n"
  "v_exp_f32 v32, v32\n"
  "v_exp_f32 v40, v40\n"
  "s_nop 0\n"
  "v_cvt_pk_bf16_f32 v33, v32, v40\n"
  "ds_write_b16 v50, v33\n"
  "ds_write_b16_d16_hi v50, v33 offset:128\n"
  "v_add_f32 v38, v32, v40\n"
  "s_add_u32 s70, s84, 0\n"
  "v_add_u32 v46, s70, v0\n"
  "v_mul_u32_u24 v47, 6554, v46\n"
  "v_lshrrev_b32 v47, 16, v47\n"
  "v_mul_u32_u24 v1, 10, v47\n"
  "v_sub_u32 v46, v46, v1\n"
  "v_lshrrev_b32 v1, 1, v47\n"
  "v_xor_b32 v1, v1, v47\n"
  "v_and_b32 v1, 4, v1\n"
  "v_mul_u32_u24 v1, 3, v1\n"
  "v_xor_b32 v47, v47, v1\n"
  "v_mul_u32_u24 v1, 1280, v47\n"
  "v_lshl_add_u32 v1, v46, 4, v1\n"
  "s_add_u32 s70, s84, 64\n"
  "v_add_u32 v46, s70, v0\n"
  "v_mul_u32_u24 v47, 6554, v46\n"
  "v_lshrrev_b32 v47, 16, v47\n"
  "v_mul_u32_u24 v2, 10, v47\n"
  "v_sub_u32 v46, v46, v2\n"
  "v_lshrrev_b32 v2, 1, v47\n"
  "v_xor_b32 v2, v2, v47\n"
  "v_and_b32 v2, 4, v2\n"
  "v_mul_u32_u24 v2, 3, v2\n"
  "v_xor_b32 v47, v47, v2\n"
  "v_mul_u32_u24 v2, 1280, v47\n"
  "v_lshl_add_u32 v2, v46, 4, v2\n"
  "s_add_u32 s70, s84, 128\n"
  "v_add_u32 v46, s70, v0\n"
  "v_mul_u32_u24 v47, 6554, v46\n"
  "v_lshrrev_b32 v47, 16, v47\n"
  "v_mul_u32_u24 v3, 10, v47\n"
  "v_sub_u32 v46, v46, v3\n"
  "v_lshrrev_b32 v3, 1, v47\n"
  "v_xor_b32 v3, v3, v47\n"
  "v_and_b32 v3, 4, v3\n"
  "v_mul_u32_u24 v3, 3, v3\n"
  "v_xor_b32 v47, v47, v3\n"
  "v_mul_u32_u24 v3, 1280, v47\n"
  "v_lshl_add_u32 v3, v46, 4, v3\n"
  "s_add_u32 s70, s84, 192\n"
  "v_add_u32 v46, s70, v0\n"
  "v_mul_u32_u24 v47, 6554, v46\n"
  "v_lshrrev_b32 v47, 16, v47\n"
  "v_mul_u32_u24 v4, 10, v47\n"
  "v_sub_u32 v46, v46, v4\n"
  "v_lshrrev_b32 v4, 1, v47\n"
  "v_xor_b32 v4, v4, v47\n"
  "v_and_b32 v4, 4, v4\n"
  "v_mul_u32_u24 v4, 3, v4\n"
  "v_xor_b32 v47, v47, v4\n"
  "v_mul_u32_u24 v4, 1280, v47\n"
  "v_lshl_add_u32 v4, v46, 4, v4\n"
  "s_add_u32 s70, s84, 256\n"
  "v_add_u32 v46, s70, v0\n"
  "v_mul_u32_u24 v47, 6554, v46\n"
  "v_lshrrev_b32 v47, 16, v47\n"
  "v_mul_u32_u24 v5, 10, v47\n"
  "v_sub_u32 v46, v46, v5\n"
  "v_lshrrev_b32 v5, 1, v47\n"
  "v_xor_b32 v5, v5, v47\n"
  "v_and_b32 v5, 4, v5\n"
  "v_mul_u32_u24 v5, 3, v5\n"
  "v_xor_b32 v47, v47, v5\n"
  "v_mul_u32_u24 v5, 1280, v47\n"
  "v_lshl_add_u32 v5, v46, 4, v5\n"
  "s_mul_i32 s74, s33, 12288\n"
  "v_lshlrev_b32 v40, 4, v0\n"
  "v_add_u32 v40, s74, v40\n"
  "v_lshrrev_b32 v45, 2, v0\n"
  "v_mul_u32_u24 v41, 384, v45\n"
  "v_and_b32 v45, 6, v45\n"
  "v_lshl_add_u32 v41, v45, 4, v41\n"
  "v_and_b32 v45, 3, v0\n"
  "v_lshl_add_u32 v41, v45, 3, v41\n"
  "v_add_u32 v41, s74, v41\n"
  "v_xor_b32 v42, 32, v41\n"
  "v_xor_b32 v43, 64, v41\n"
  "v_xor_b32 v44, 96, v41\n"
  "buffer_load_dwordx4 v[76:79], v222, s[16:19], s72 offen nt\n"
  "s_add_u32 s72, s95, 15360\n"
  "buffer_load_dwordx4 v[84:87], v220, s[16:19], s72 offen nt\n"
  "buffer_load_dwordx4 v[92:95], v221, s[16:19], s72 offen nt\n"
  "buffer_load_dwordx4 v[100:103], v222, s[16:19], s72 offen nt\n"
  "s_add_u32 s72, s95, 18432\n"
  "buffer_load_dwordx4 v[108:111], v220, s[16:19], s72 offen nt\n"
  "buffer_load_dwordx4 v[116:119], v221, s[16:19], s72 offen nt\n"
  "buffer_load_dwordx4 v[124:127], v222, s[16:19], s72 offen nt\n"
  "s_add_u32 s72, s95, 21504\n"
  "buffer_load_dwordx4 v[132:135], v220, s[16:19], s72 offen nt\n"
  "buffer_load_dwordx4 v[140:143], v221, s[16:19], s72 offen nt\n"
  "buffer_load_dwordx4 v[148:151], v222, s[16:19], s72 offen nt\n"
  "s_add_u32 s72, s95, 24576\n"
  "buffer_load_dwordx4 v[152:155], v220, s[16:19], s72 offen nt\n"
  "buffer_load_dwordx4 v[160:163], v221, s[16:19], s72 offen nt\n"
  "LCV0:\n"
  "s_waitcnt vmcnt(25)\n"
  "ds_write_b128 v40, v[56:59] offset:0\n"
  "s_waitcnt vmcnt(24)\n"
  "ds_write_b128 v40, v[64:67] offset:1024\n"
  "s_waitcnt vmcnt(23)\n"
  "ds_write_b128 v40, v[72:75] offset:2048\n"
  "s_waitcnt vmcnt(22)\n"
  "ds_write_b128 v40, v[80:83] offset:3072\n"
  "s_waitcnt vmcnt(21)\n"
  "ds_write_b128 v40, v[88:91] offset:4096\n"
  "s_waitcnt vmcnt(20)\n"
  "ds_write_b128 v40, v[96:99] offset:5120\n"
  "s_waitcnt vmcnt(19)\n"
  "ds_write_b128 v40, v[104:107] offset:6144\n"
  "s_waitcnt vmcnt(18)\n"
  "ds_write_b128 v40, v[112:115] offset:7168\n"
  "s_waitcnt vmcnt(17)\n"
  "ds_write_b128 v40, v[120:123] offset:8192\n"
  "s_waitcnt vmcnt(16)\n"
  "ds_write_b128 v40, v[128:131] offset:9216\n"
  "s_waitcnt vmcnt(15)\n"
  "ds_write_b128 v40, v[136:139] offset:10240\n"
  "s_waitcnt vmcnt(14)\n"
  "ds_write_b128 v40, v[144:147] offset:11264\n"
  "s_waitcnt lgkmcnt(0)\n"
  "ds_read_b128 v[6:9], v51 offset:0\n"
  "ds_read_b64_tr_b16 v[56:57], v41 offset:0\n"
  "ds_read_b64_tr_b16 v[58:59], v41 offset:6144\n"
  "ds_read_b64_tr_b16 v[88:89], v41 offset:128\n"
  "ds_read_b64_tr_b16 v[90:91], v41 offset:6272\n"
  "ds_read_b64_tr_b16 v[120:121], v41 offset:256\n"
  "ds_read_b64_tr_b16 v[122:123], v41 offset:6400\n"
  "ds_read_b128 v[10:13], v52 offset:0\n"
  "ds_read_b64_tr_b16 v[64:65], v42 offset:0\n"
  "ds_read_b64_tr_b16 v[66:67], v42 offset:6144\n"
  "ds_read_b64_tr_b16 v[96:97], v42 offset:128\n"
  "ds_read_b64_tr_b16 v[98:99], v42 offset:6272\n"
  "ds_read_b64_tr_b16 v[128:129], v42 offset:256\n"
  "ds_read_b64_tr_b16 v[130:131], v42 offset:6400\n"
  "s_waitcnt lgkmcnt(7)\n"
  "v_mfma_f32_16x16x32_bf16 v[22:25], v[6:9], v[56:59], 0\n"
  "v_mfma_f32_16x16x32_bf16 v[26:29], v[6:9], v[88:91], 0\n"
  "v_mfma_f32_16x16x32_bf16 v[30:33], v[6:9], v[120:123], 0\n"
  "v_add_f32_dpp v38, v38, v38 quad_perm:[1,0,3,2] row_mask:0xf bank_mask:0xf\n"
  "ds_read_b128 v[14:17], v53 offset:0\n"
  "ds_read_b64_tr_b16 v[72:73], v43 offset:0\n"
  "ds_read_b64_tr_b16 v[74:75], v43 offset:6144\n"
  "ds_read_b64_tr_b16 v[104:105], v43 offset:128\n"
  "ds_read_b64_tr_b16 v[106:107], v43 offset:6272\n"
  "ds_read_b64_tr_b16 v[136:137], v43 offset:256\n"
  "ds_read_b64_tr_b16 v[138:139], v43 offset:6400\n"
  "s_waitcnt lgkmcnt(7)\n"
  "v_mfma_f32_16x16x32_bf16 v[22:25], v[10:13], v[64:67], v[22:25]\n"
  "v_mfma_f32_16x16x32_bf16 v[26:29], v[10:13], v[96:99], v[26:29]\n"
  "v_mfma_f32_16x16x32_bf16 v[30:33], v[10:13], v[128:131], v[30:33]\n"
  "v_add_f32_dpp v38, v38, v38 quad_perm:[2,3,0,1] row_mask:0xf bank_mask:0xf\n"
  "ds_read_b128 v[18:21], v54 offset:0\n"
  "ds_read_b64_tr_b16 v[80:81], v44 offset:0\n"
  "ds_read_b64_tr_b16 v[82:83], v44 offset:6144\n"
  "ds_read_b64_tr_b16 v[112:113], v44 offset:128\n"
  "ds_read_b64_tr_b16 v[114:115], v44 offset:6272\n"
  "ds_read_b64_tr_b16 v[144:145], v44 offset:256\n"
  "ds_read_b64_tr_b16 v[146:147], v44 offset:6400\n"
  "s_waitcnt lgkmcnt(7)\n"
  "v_mfma_f32_16x16x32_bf16 v[22:25], v[14:17], v[72:75], v[22:25]\n"
  "v_mfma_f32_16x16x32_bf16 v[26:29], v[14:17], v[104:107], v[26:29]\n"
  "v_mfma_f32_16x16x32_bf16 v[30:33], v[14:17], v[136:139], v[30:33]\n"
  "v_add_f32_dpp v38, v38, v38 row_ror:4 row_mask:0xf bank_mask:0xf\n"
  "s_waitcnt lgkmcnt(0)\n"
  "v_mfma_f32_16x16x32_bf16 v[22:25], v[18:21], v[80:83], v[22:25]\n"
  "v_mfma_f32_16x16x32_bf16 v[26:29], v[18:21], v[112:115], v[26:29]\n"
  "v_mfma_f32_16x16x32_bf16 v[30:33], v[18:21], v[144:147], v[30:33]\n"
  "v_add_f32_dpp v38, v38, v38 row_ror:8 row_mask:0xf bank_mask:0xf\n"
  "buffer_load_dwordx4 v[168:171], v222, s[16:19], s72 offen nt\n"
  "s_add_u32 s72, s95, 27648\n"
  "buffer_load_dwordx4 v[176:179], v220, s[16:19], s72 offen nt\n"
  "buffer_load_dwordx4 v[184:187], v221, s[16:19], s72 offen nt\n"
  "buffer_load_dwordx4 v[192:195], v222, s[16:19], s72 offen nt\n"
  "s_add_u32 s72, s95, 30720\n"
  "buffer_load_dwordx4 v[200:203], v220, s[16:19], s72 offen nt\n"
  "buffer_load_dwordx4 v[208:211], v221, s[16:19], s72 offen nt\n"
  "buffer_load_dwordx4 a[224:227], v222, s[16:19], s72 offen nt\n"
  "s_add_u32 s72, s95, 33792\n"
  "buffer_load_dwordx4 a[232:235], v220, s[16:19], s72 offen nt\n"
  "buffer_load_dwordx4 a[240:243], v221, s[16:19], s72 offen nt\n"
  "buffer_load_dwordx4 a[248:251], v222, s[16:19], s72 offen nt\n"
  "s_add_u32 s72, s95, 36864\n"
  "buffer_load_dwordx4 v[156:159], v220, s[16:19], s72 offen nt\n"
  "buffer_load_dwordx4 v[164:167], v221, s[16:19], s72 offen nt\n"
  "LCV1:\n"
  "s_waitcnt vmcnt(25)\n"
  "ds_write_b128 v40, v[60:63] offset:0\n"
  "s_waitcnt vmcnt(24)\n"
  "ds_write_b128 v40, v[68:71] offset:1024\n"
  "s_waitcnt vmcnt(23)\n"
  "ds_write_b128 v40, v[76:79] offset:2048\n"
  "s_waitcnt vmcnt(22)\n"
  "ds_write_b128 v40, v[84:87] offset:3072\n"
  "s_waitcnt vmcnt(21)\n"
  "ds_write_b128 v40, v[92:95] offset:4096\n"
  "s_waitcnt vmcnt(20)\n"
  "ds_write_b128 v40, v[100:103] offset:5120\n"
  "s_waitcnt vmcnt(19)\n"
  "ds_write_b128 v40, v[108:111] offset:6144\n"
  "s_waitcnt vmcnt(18)\n"
  "ds_write_b128 v40, v[116:119] offset:7168\n"
  "s_waitcnt vmcnt(17)\n"
  "ds_write_b128 v40, v[124:127] offset:8192\n"
  "s_waitcnt vmcnt(16)\n"
  "ds_write_b128 v40, v[132:135] offset:9216\n"
  "s_waitcnt vmcnt(15)\n"
  "ds_write_b128 v40, v[140:143] offset:10240\n"
  "s_waitcnt vmcnt(14)\n"
  "ds_write_b128 v40, v[148:151] offset:11264\n"
  "s_waitcnt lgkmcnt(0)\n"
  "ds_read_b128 v[6:9], v51 offset:64\n"
  "ds_read_b64_tr_b16 v[60:61], v41 offset:0\n"
  "ds_read_b64_tr_b16 v[62:63], v41 offset:6144\n"
  "ds_read_b64_tr_b16 v[92:93], v41 offset:128\n"
  "ds_read_b64_tr_b16 v[94:95], v41 offset:6272\n"
  "ds_read_b64_tr_b16 v[124:125], v41 offset:256\n"
  "ds_read_b64_tr_b16 v[126:127], v41 offset:6400\n"
  "ds_read_b128 v[10:13], v52 offset:64\n"
  "ds_read_b64_tr_b16 v[68:69], v42 offset:0\n"
  "ds_read_b64_tr_b16 v[70:71], v42 offset:6144\n"
  "ds_read_b64_tr_b16 v[100:101], v42 offset:128\n"
  "ds_read_b64_tr_b16 v[102:103], v42 offset:6272\n"
  "ds_read_b64_tr_b16 v[132:133], v42 offset:256\n"
  "ds_read_b64_tr_b16 v[134:135], v42 offset:6400\n"
  "s_waitcnt lgkmcnt(7)\n"
  "v_mfma_f32_16x16x32_bf16 v[22:25], v[6:9], v[60:63], v[22:25]\n"
  "v_mfma_f32_16x16x32_bf16 v[26:29], v[6:9], v[92:95], v[26:29]\n"
  "v_mfma_f32_16x16x32_bf16 v[30:33], v[6:9], v[124:127], v[30:33]\n"
  "v_mov_b32 v47, v38\n"
  "s_nop 1\n"
  "v_permlane16_swap_b32 v38, v47\n"
  "v_add_f32 v38, v38, v47\n"
  "ds_read_b128 v[14:17], v53 offset:64\n"
  "ds_read_b64_tr_b16 v[76:77], v43 offset:0\n"
  "ds_read_b64_tr_b16 v[78:79], v43 offset:6144\n"
  "ds_read_b64_tr_b16 v[108:109], v43 offset:128\n"
  "ds_read_b64_tr_b16 v[110:111], v43 offset:6272\n"
  "ds_read_b64_tr_b16 v[140:141], v43 offset:256\n"
  "ds_read_b64_tr_b16 v[142:143], v43 offset:6400\n"
  "s_waitcnt lgkmcnt(7)\n"
  "v_mfma_f32_16x16x32_bf16 v[22:25], v[10:13], v[68:71], v[22:25]\n"
  "v_mfma_f32_16x16x32_bf16 v[26:29], v[10:13], v[100:103], v[26:29]\n"
  "v_mfma_f32_16x16x32_bf16 v[30:33], v[10:13], v[132:135], v[30:33]\n"
  "v_mov_b32 v47, v38\n"
  "s_nop 1\n"
  "v_permlane32_swap_b32 v38, v47\n"
  "v_add_f32 v38, v38, v47\n"
  "ds_read_b128 v[18:21], v54 offset:64\n"
  "ds_read_b64_tr_b16 v[84:85], v44 offset:0\n"
  "ds_read_b64_tr_b16 v[86:87], v44 offset:6144\n"
  "ds_read_b64_tr_b16 v[116:117], v44 offset:128\n"
  "ds_read_b64_tr_b16 v[118:119], v44 offset:6272\n"
  "ds_read_b64_tr_b16 v[148:149], v44 offset:256\n"
  "ds_read_b64_tr_b16 v[150:151], v44 offset:6400\n"
  "s_waitcnt lgkmcnt(7)\n"
  "v_mfma_f32_16x16x32_bf16 v[22:25], v[14:17], v[76:79], v[22:25]\n"
  "v_mfma_f32_16x16x32_bf16 v[26:29], v[14:17], v[108:111], v[26:29]\n"
  "v_mfma_f32_16x16x32_bf16 v[30:33], v[14:17], v[140:143], v[30:33]\n"
  "s_waitcnt lgkmcnt(0)\n"
  "v_mfma_f32_16x16x32_bf16 v[22:25], v[18:21], v[84:87], v[22:25]\n"
  "v_mfma_f32_16x16x32_bf16 v[26:29], v[18:21], v[116:119], v[26:29]\n"
  "v_mfma_f32_16x16x32_bf16 v[30:33], v[18:21], v[148:151], v[30:33]\n"
  "buffer_load_dwordx4 v[172:175], v222, s[16:19], s72 offen nt\n"
  "s_add_u32 s72, s95, 39936\n"
  "buffer_load_dwordx4 v[180:183], v220, s[16:19], s72 offen nt\n"
  "buffer_load_dwordx4 v[188:191], v221, s[16:19], s72 offen nt\n"
  "buffer_load_dwordx4 v[196:199], v222, s[16:19], s72 offen nt\n"
  "s_add_u32 s72, s95, 43008\n"
  "buffer_load_dwordx4 v[204:207], v220, s[16:19], s72 offen nt\n"
  "buffer_load_dwordx4 v[212:215], v221, s[16:19], s72 offen nt\n"
  "buffer_load_dwordx4 a[228:231], v222, s[16:19], s72 offen nt\n"
  "s_add_u32 s72, s95, 46080\n"
  "buffer_load_dwordx4 a[236:239], v220, s[16:19], s72 offen nt\n"
  "buffer_load_dwordx4 a[244:247], v221, s[16:19], s72 offen nt\n"
  "buffer_load_dwordx4 a[252:255], v222, s[16:19], s72 offen nt\n"
  "s_lshl_b32 s70, s84, 4\n"
  "s_add_u32 m0, s70, 129024\n"
  "s_add_u32 s71, s82, 0\n"
  "buffer_load_dwordx4 v1, s[24:27], s71 offen  lds\n"
  "s_lshl_b32 s70, s84, 4\n"
  "s_add_u32 m0, s70, 130048\n"
  "s_add_u32 s71, s82, 0\n"
  "buffer_load_dwordx4 v2, s[24:27], s71 offen  lds\n"
  "LCV2:\n"
  "s_waitcnt vmcnt(25)\n"
  "ds_write_b128 v40, v[152:155] offset:0\n"
  "s_waitcnt vmcnt(24)\n"
  "ds_write_b128 v40, v[160:163] offset:1024\n"
  "s_waitcnt vmcnt(23)\n"
  "ds_write_b128 v40, v[168:171] offset:2048\n"
  "s_waitcnt vmcnt(22)\n"
  "ds_write_b128 v40, v[176:179] offset:3072\n"
  "s_waitcnt vmcnt(21)\n"
  "ds_write_b128 v40, v[184:187] offset:4096\n"
  "s_waitcnt vmcnt(20)\n"
  "ds_write_b128 v40, v[192:195] offset:5120\n"
  "s_waitcnt vmcnt(19)\n"
  "ds_write_b128 v40, v[200:203] offset:6144\n"
  "s_waitcnt vmcnt(18)\n"
  "ds_write_b128 v40, v[208:211] offset:7168\n"
  "s_waitcnt vmcnt(17)\n"
  "ds_write_b128 v40, a[224:227] offset:8192\n"
  "s_waitcnt vmcnt(16)\n"
  "ds_write_b128 v40, a[232:235] offset:9216\n"
  "s_waitcnt vmcnt(15)\n"
  "ds_write_b128 v40, a[240:243] offset:10240\n"
  "s_waitcnt vmcnt(14)\n"
  "ds_write_b128 v40, a[248:251] offset:11264\n"
  "s_waitcnt lgkmcnt(0)\n"
  "ds_read_b128 v[6:9], v51 offset:128\n"
  "ds_read_b64_tr_b16 v[152:153], v41 offset:0\n"
  "ds_read_b64_tr_b16 v[154:155], v41 offset:6144\n"
  "ds_read_b64_tr_b16 v[184:185], v41 offset:128\n"
  "ds_read_b64_tr_b16 v[186:187], v41 offset:6272\n"
  "ds_read_b64_tr_b16 a[224:225], v41 offset:256\n"
  "ds_read_b64_tr_b16 a[226:227], v41 offset:6400\n"
  "ds_read_b128 v[10:13], v52 offset:128\n"
  "ds_read_b64_tr_b16 v[160:161], v42 offset:0\n"
  "ds_read_b64_tr_b16 v[162:163], v42 offset:6144\n"
  "ds_read_b64_tr_b16 v[192:193], v42 offset:128\n"
  "ds_read_b64_tr_b16 v[194:195], v42 offset:6272\n"
  "ds_read_b64_tr_b16 a[232:233], v42 offset:256\n"
  "ds_read_b64_tr_b16 a[234:235], v42 offset:6400\n"
  "s_waitcnt lgkmcnt(7)\n"
  "v_mfma_f32_16x16x32_bf16 v[22:25], v[6:9], v[152:155], v[22:25]\n"
  "v_mfma_f32_16x16x32_bf16 v[26:29], v[6:9], v[184:187], v[26:29]\n"
  "v_mfma_f32_16x16x32_bf16 v[30:33], v[6:9], a[224:227], v[30:33]\n"
  "ds_read_b128 v[14:17], v53 offset:128\n"
  "ds_read_b64_tr_b16 v[168:169], v43 offset:0\n"
  "ds_read_b64_tr_b16 v[170:171], v43 offset:6144\n"
  "ds_read_b64_tr_b16 v[200:201], v43 offset:128\n"
  "ds_read_b64_tr_b16 v[202:203], v43 offset:6272\n"
  "ds_read_b64_tr_b16 a[240:241], v43 offset:256\n"
  "ds_read_b64_tr_b16 a[242:243], v43 offset:6400\n"
  "s_waitcnt lgkmcnt(7)\n"
  "v_mfma_f32_16x16x32_bf16 v[22:25], v[10:13], v[160:163], v[22:25]\n"
  "v_mfma_f32_16x16x32_bf16 v[26:29], v[10:13], v[192:195], v[26:29]\n"
  "v_mfma_f32_16x16x32_bf16 v[30:33], v[10:13], a[232:235], v[30:33]\n"
  "ds_read_b128 v[18:21], v54 offset:128\n"
  "ds_read_b64_tr_b16 v[176:177], v44 offset:0\n"
  "ds_read_b64_tr_b16 v[178:179], v44 offset:6144\n"
  "ds_read_b64_tr_b16 v[208:209], v44 offset:128\n"
  "ds_read_b64_tr_b16 v[210:211], v44 offset:6272\n"
  "ds_read_b64_tr_b16 a[248:249], v44 offset:256\n"
  "ds_read_b64_tr_b16 a[250:251], v44 offset:6400\n"
  "s_waitcnt lgkmcnt(7)\n"
  "v_mfma_f32_16x16x32_bf16 v[22:25], v[14:17], v[168:171], v[22:25]\n"
  "v_mfma_f32_16x16x32_bf16 v[26:29], v[14:17], v[200:203], v[26:29]\n"
  "v_mfma_f32_16x16x32_bf16 v[30:33], v[14:17], a[240:243], v[30:33]\n"
  "s_waitcnt lgkmcnt(0)\n"
  "v_mfma_f32_16x16x32_bf16 v[22:25], v[18:21], v[176:179], v[22:25]\n"
  "v_mfma_f32_16x16x32_bf16 v[26:29], v[18:21], v[208:211], v[26:29]\n"
  "v_mfma_f32_16x16x32_bf16 v[30:33], v[18:21], a[248:251], v[30:33]\n"
  "s_lshl_b32 s70, s84, 4\n"
  "s_add_u32 m0, s70, 131072\n"
  "s_add_u32 s71, s82, 0\n"
  "buffer_load_dwordx4 v3, s[24:27], s71 offen  lds\n"
  "s_lshl_b32 s70, s84, 4\n"
  "s_add_u32 m0, s70, 132096\n"
  "s_add_u32 s71, s82, 0\n"
  "buffer_load_dwordx4 v4, s[24:27], s71 offen  lds\n"
  "s_lshl_b32 s70, s84, 4\n"
  "s_add_u32 m0, s70, 133120\n"
  "s_add_u32 s71, s82, 0\n"
  "buffer_load_dwordx4 v5, s[24:27], s71 offen  lds\n"
  "s_lshl_b32 s70, s84, 4\n"
  "s_add_u32 m0, s70, 134144\n"
  "s_add_u32 s71, s82, 40960\n"
  "buffer_load_dwordx4 v1, s[24:27], s71 offen  lds\n"
  "s_lshl_b32 s70, s84, 4\n"
  "s_add_u32 m0, s70, 135168\n"
  "s_add_u32 s71, s82, 40960\n"
  "buffer_load_dwordx4 v2, s[24:27], s71 offen  lds\n"
  "s_lshl_b32 s70, s84, 4\n"
  "s_add_u32 m0, s70, 136192\n"
  "s_add_u32 s71, s82, 40960\n"
  "s_mov_b32 exec_lo, 0xffffffff\n"
  "s_mov_b32 exec_hi, 0x0\n"
  "buffer_load_dwordx4 v3, s[24:27], s71 offen  lds\n"
  "s_mov_b64 exec, -1\n"
  "LCV3:\n"
  "s_waitcnt vmcnt(19)\n"
  "ds_write_b128 v40, v[156:159] offset:0\n"
  "s_waitcnt vmcnt(18)\n"
  "ds_write_b128 v40, v[164:167] offset:1024\n"
  "s_waitcnt vmcnt(17)\n"
  "ds_write_b128 v40, v[172:175] offset:2048\n"
  "s_waitcnt vmcnt(16)\n"
  "ds_write_b128 v40, v[180:183] offset:3072\n"
  "s_waitcnt vmcnt(15)\n"
  "ds_write_b128 v40, v[188:191] offset:4096\n"
  "s_waitcnt vmcnt(14)\n"
  "ds_write_b128 v40, v[196:199] offset:5120\n"
  "s_waitcnt vmcnt(13)\n"
  "ds_write_b128 v40, v[204:207] offset:6144\n"
  "s_waitcnt vmcnt(12)\n"
  "ds_write_b128 v40, v[212:215] offset:7168\n"
  "s_waitcnt vmcnt(11)\n"
  "ds_write_b128 v40, a[228:231] offset:8192\n"
  "s_waitcnt vmcnt(10)\n"
  "ds_write_b128 v40, a[236:239] offset:9216\n"
  "s_waitcnt vmcnt(9)\n"
  "ds_write_b128 v40, a[244:247] offset:10240\n"
  "s_waitcnt vmcnt(8)\n"
  "ds_write_b128 v40, a[252:255] offset:11264\n"
  "s_waitcnt lgkmcnt(0)\n"
  "ds_read_b128 v[6:9], v51 offset:192\n"
  "ds_read_b64_tr_b16 v[156:157], v41 offset:0\n"
  "ds_read_b64_tr_b16 v[158:159], v41 offset:6144\n"
  "ds_read_b64_tr_b16 v[188:189], v41 offset:128\n"
  "ds_read_b64_tr_b16 v[190:191], v41 offset:6272\n"
  "ds_read_b64_tr_b16 a[228:229], v41 offset:256\n"
  "ds_read_b64_tr_b16 a[230:231], v41 offset:6400\n"
  "ds_read_b128 v[10:13], v52 offset:192\n"
  "ds_read_b64_tr_b16 v[164:165], v42 offset:0\n"
  "ds_read_b64_tr_b16 v[166:167], v42 offset:6144\n"
  "ds_read_b64_tr_b16 v[196:197], v42 offset:128\n"
  "ds_read_b64_tr_b16 v[198:199], v42 offset:6272\n"
  "ds_read_b64_tr_b16 a[236:237], v42 offset:256\n"
  "ds_read_b64_tr_b16 a[238:239], v42 offset:6400\n"
  "s_waitcnt lgkmcnt(7)\n"
  "v_mfma_f32_16x16x32_bf16 v[22:25], v[6:9], v[156:159], v[22:25]\n"
  "v_mfma_f32_16x16x32_bf16 v[26:29], v[6:9], v[188:191], v[26:29]\n"
  "v_mfma_f32_16x16x32_bf16 v[30:33], v[6:9], a[228:231], v[30:33]\n"
  "ds_read_b128 v[14:17], v53 offset:192\n"
  "ds_read_b64_tr_b16 v[172:173], v43 offset:0\n"
  "ds_read_b64_tr_b16 v[174:175], v43 offset:6144\n"
  "ds_read_b64_tr_b16 v[204:205], v43 offset:128\n"
  "ds_read_b64_tr_b16 v[206:207], v43 offset:6272\n"
  "ds_read_b64_tr_b16 a[244:245], v43 offset:256\n"
  "ds_read_b64_tr_b16 a[246:247], v43 offset:6400\n"
  "s_waitcnt lgkmcnt(7)\n"
  "v_mfma_f32_16x16x32_bf16 v[22:25], v[10:13], v[164:167], v[22:25]\n"
  "v_mfma_f32_16x16x32_bf16 v[26:29], v[10:13], v[196:199], v[26:29]\n"
  "v_mfma_f32_16x16x32_bf16 v[30:33], v[10:13], a[236:239], v[30:33]\n"
  "ds_read_b128 v[18:21], v54 offset:192\n"
  "ds_read_b64_tr_b16 v[180:181], v44 offset:0\n"
  "ds_read_b64_tr_b16 v[182:183], v44 offset:6144\n"
  "ds_read_b64_tr_b16 v[212:213], v44 offset:128\n"
  "ds_read_b64_tr_b16 v[214:215], v44 offset:6272\n"
  "ds_read_b64_tr_b16 a[252:253], v44 offset:256\n"
  "ds_read_b64_tr_b16 a[254:255], v44 offset:6400\n"
  "s_waitcnt lgkmcnt(7)\n"
  "v_mfma_f32_16x16x32_bf16 v[22:25], v[14:17], v[172:175], v[22:25]\n"
  "v_mfma_f32_16x16x32_bf16 v[26:29], v[14:17], v[204:207], v[26:29]\n"
  "v_mfma_f32_16x16x32_bf16 v[30:33], v[14:17], a[244:247], v[30:33]\n"
  "s_waitcnt lgkmcnt(0)\n"
  "v_mfma_f32_16x16x32_bf16 v[22:25], v[18:21], v[180:183], v[22:25]\n"
  "v_mfma_f32_16x16x32_bf16 v[26:29], v[18:21], v[212:215], v[26:29]\n"
  "v_mfma_f32_16x16x32_bf16 v[30:33], v[18:21], a[252:255], v[30:33]\n"
  "s_lshl_b32 s70, s33, 10\n"
  "v_lshlrev_b32 v40, 4, v0\n"
  "v_add_u32 v40, 90112, v40\n"
  "v_add_u32 v41, s70, v40\n"
  "s_lshl_b32 s70, s33, 2\n"
  "s_add_u32 s70, s70, 162496\n"
  "v_mov_b32 v42, s70\n"
  "v_mov_b32 v43, 162496\n"
  "v_lshlrev_b32 v44, 3, v0\n"
  "s_mov_b32 s66, 2\n"
  "s_mov_b32 s67, 0\n"
  "v_mov_b32 v45, 0x200\n"
  "v_cndmask_b32_e64 v44, v44, v45, s[66:67]\n"
  "s_lshl_b32 s76, s83, 10\n"
  "s_add_u32 s76, s76, 1179648\n"
  "ds_write_b32 v42, v38\n"
  "v_mov_b32 v36, v22\n"
  "v_mov_b32 v37, v26\n"
  "v_mov_b32 v38, v30\n"
  "ds_write_b128 v41, v[36:39]\n"
  "s_waitcnt lgkmcnt(0)\n"
  "s_barrier\n"
  "s_cmp_lg_u32 s33, 3\n"
  "s_cbranch_scc1 Lsk_3\n"
  "ds_read_b128 v[6:9], v40 offset:0\n"
  "ds_read_b128 v[10:13], v40 offset:1024\n"
  "ds_read_b128 v[14:17], v40 offset:2048\n"
  "ds_read_b128 v[18:21], v43\n"
  "v_mov_b32 v25, s62\n"
  "s_waitcnt lgkmcnt(0)\n"
  "v_max3_f32 v26, v9, v13, v17\n"
  "v_max_f32 v26, v26, v39\n"
  "v_sub_f32 v9, v9, v26\n"
  "v_sub_f32 v13, v13, v26\n"
  "v_sub_f32 v17, v17, v26\n"
  "v_sub_f32 v39, v39, v26\n"
  "v_exp_f32 v9, v9\n"
  "v_exp_f32 v13, v13\n"
  "v_exp_f32 v17, v17\n"
  "v_exp_f32 v39, v39\n"
  "v_mul_f32 v6, v9, v6\n"
  "v_mul_f32 v7, v9, v7\n"
  "v_mul_f32 v8, v9, v8\n"
  "v_fmac_f32 v6, v13, v10\n"
  "v_fmac_f32 v7, v13, v11\n"
  "v_fmac_f32 v8, v13, v12\n"
  "v_fmac_f32 v6, v17, v14\n"
  "v_fmac_f32 v7, v17, v15\n"
  "v_fmac_f32 v8, v17, v16\n"
  "v_fmac_f32 v6, v39, v36\n"
  "v_fmac_f32 v7, v39, v37\n"
  "v_fmac_f32 v8, v39, v38\n"
  "v_mul_f32 v18, v9, v18\n"
  "v_fmac_f32 v18, v13, v19\n"
  "v_mov_b32_dpp v10, v6 quad_perm:[1,0,3,2] row_mask:0xf bank_mask:0xf\n"
  "v_mov_b32_dpp v11, v7 quad_perm:[1,0,3,2] row_mask:0xf bank_mask:0xf\n"
  "v_mov_b32_dpp v12, v8 quad_perm:[1,0,3,2] row_mask:0xf bank_mask:0xf\n"
  "v_fmac_f32 v18, v17, v20\n"
  "v_fmac_f32 v18, v39, v21\n"
  "v_cvt_pk_bf16_f32 v22, v6, v10\n"
  "v_cvt_pk_bf16_f32 v23, v7, v11\n"
  "v_cvt_pk_bf16_f32 v24, v8, v12\n"
  "v_cndmask_b32_e64 v22, v22, v26, s[66:67]\n"
  "v_cndmask_b32_e64 v23, v23, v18, s[66:67]\n"
  "s_mov_b32 exec_lo, 0x55555557\n"
  "s_mov_b32 exec_hi, 0x55555555\n"
  "buffer_store_dwordx4 v[22:25], v44, s[56:59], s76 offen sc1\n"
  "s_mov_b64 exec, -1\n"
  "Lsk_3:\n"
  "v_lshrrev_b32 v54, 5, v0\n"
  "v_lshlrev_b32 v47, 10, v54\n"
  "v_and_b32 v48, 31, v0\n"
  "v_lshl_add_u32 v46, v48, 4, v47\n"
  "v_add_u32 v47, 0x200, v47\n"
  "s_lshl_b32 s70, s33, 1\n"
  "v_add_u32 v54, s70, v54\n"
  "s_lshl_b32 s71, s34, 3\n"
  "s_add_u32 s70, s70, s71\n"
  "s_lshl_b32 s70, s70, 10\n"
  "s_add_u32 s88, s70, 1179648\n"
  "v_lshlrev_b32 v49, 3, v54\n"
  "v_add_u32 v49, 162432, v49\n"
  "v_mul_u32_u24 v54, 400, v54\n"
  "v_lshl_add_u32 v48, v48, 2, v54\n"
  "v_add_u32 v48, 94208, v48\n"
  "v_and_b32 v50, 7, v0\n"
  "v_mul_u32_u24 v50, 400, v50\n"
  "v_and_b32 v54, 0x30, v0\n"
  "v_add_u32 v50, v50, v54\n"
  "v_add_u32 v50, 94208, v50\n"
  "v_and_b32 v54, 3, v0\n"
  "v_and_b32 v52, 16, v0\n"
  "v_lshrrev_b32 v52, 2, v52\n"
  "v_add_u32 v52, v52, v54\n"
  "v_lshlrev_b32 v52, 3, v52\n"
  "v_add_u32 v52, 162432, v52\n"
  "v_and_b32 v53, 15, v0\n"
  "v_mul_u32_u24 v53, 43, v53\n"
  "v_lshrrev_b32 v53, 8, v53\n"
  "v_lshrrev_b32 v54, 4, v0\n"
  "v_mul_u32_u24 v54, 3, v54\n"
  "s_mul_i32 s71, s33, 6\n"
  "v_add3_u32 v53, v53, v54, s71\n"
  "v_lshrrev_b32 v54, 2, v53\n"
  "v_and_b32 v53, 3, v53\n"
  "v_lshlrev_b32 v53, 4, v53\n"
  "v_lshl_add_u32 v53, v54, 11, v53\n"
  "s_lshl_b32 s70, s35, 7\n"
  "s_add_u32 s70, s70, s34\n"
  "s_lshl_b32 s70, s70, 6\n"
  "s_add_u32 s76, s70, 917504\n"
  "v_lshrrev_b32 v51, 2, v0\n"
  "v_and_b32 v51, 7, v51\n"
  "v_and_b32 v54, 32, v0\n"
  "v_lshrrev_b32 v54, 1, v54\n"
  "v_add_u32 v51, v51, v54\n"
  "v_mul_u32_u24 v51, 160, v51\n"
  "v_and_b32 v54, 3, v0\n"
  "v_lshl_add_u32 v51, v54, 3, v51\n"
  "s_lshl_b32 s70, s33, 6\n"
  "s_add_u32 s70, s70, 129024\n"
  "v_add_u32 v51, s70, v51\n"
  "LCA2:\n"
  "s_lshl_b32 s99, s33, 3\n"
  "s_lshr_b32 s98, 0x282c2c2c, s99\n"
  "s_and_b32 s98, s98, 0xff\n"
  "Ldl_4:\n"
  "s_sub_u32 s98, s98, 1\n"
  "s_cbranch_scc0 Ldl_4\n"
  "buffer_load_dwordx4 v[10:13], v47, s[56:59], s88 offen sc1\n"
  "buffer_load_dwordx4 v[6:9], v46, s[56:59], s88 offen sc1\n"
  "LPA2_5:\n"
  "s_waitcnt vmcnt(1)\n"
  "v_cmp_ne_u32_e64 s[64:65], s62, v13\n"
  "s_waitcnt vmcnt(0)\n"
  "v_cmp_ne_u32 vcc, s62, v9\n"
  "s_nop 0\n"
  "s_or_b64 s[64:65], s[64:65], vcc\n"
  "s_cbranch_scc1 LSA2_6\n"
  "ds_write2_b32 v48, v6, v7 offset0:0 offset1:32\n"
  "ds_write_b32 v48, v8 offset:256\n"
  "ds_write_b64 v49, v[10:11]\n"
  "s_waitcnt lgkmcnt(0)\n"
  "s_barrier\n"
  "s_cmp_eq_u32 s33, 3\n"
  "s_cbranch_scc1 Lsk_7\n"
  "ds_read_b64 v[46:47], v52\n"
  "ds_read_b128 v[6:9], v50 offset:0\n"
  "ds_read_b64_tr_b16 v[18:19], v51 offset:0\n"
  "ds_read_b64_tr_b16 v[20:21], v51 offset:1280\n"
  "ds_read_b64_tr_b16 v[22:23], v51 offset:32\n"
  "ds_read_b64_tr_b16 v[24:25], v51 offset:1312\n"
  "ds_read_b128 v[10:13], v50 offset:64\n"
  "ds_read_b64_tr_b16 v[26:27], v51 offset:5120\n"
  "ds_read_b64_tr_b16 v[28:29], v51 offset:6400\n"
  "ds_read_b64_tr_b16 v[30:31], v51 offset:5152\n"
  "ds_read_b64_tr_b16 v[32:33], v51 offset:6432\n"
  "s_waitcnt lgkmcnt(5)\n"
  "ds_read_b128 v[14:17], v50 offset:128\n"
  "ds_read_b64_tr_b16 v[34:35], v51 offset:10240\n"
  "ds_read_b64_tr_b16 v[36:37], v51 offset:11520\n"
  "ds_read_b64_tr_b16 v[38:39], v51 offset:10272\n"
  "ds_read_b64_tr_b16 v[40:41], v51 offset:11552\n"
  "v_mfma_f32_16x16x32_bf16 v[42:45], v[6:9], v[18:21], 0\n"
  "v_mfma_f32_16x16x32_bf16 v[2:5], v[6:9], v[22:25], 0\n"
  "v_mov_b32 v48, v46\n"
  "s_nop 1\n"
  "v_max_f32_dpp v48, v48, v48 quad_perm:[1,0,3,2] row_mask:0xf bank_mask:0xf\n"
  "s_waitcnt lgkmcnt(5)\n"
  "ds_read_b128 v[6:9], v50 offset:192\n"
  "ds_read_b64_tr_b16 v[18:19], v51 offset:15360\n"
  "ds_read_b64_tr_b16 v[20:21], v51 offset:16640\n"
  "ds_read_b64_tr_b16 v[22:23], v51 offset:15392\n"
  "ds_read_b64_tr_b16 v[24:25], v51 offset:16672\n"
  "v_mfma_f32_16x16x32_bf16 v[42:45], v[10:13], v[26:29], v[42:45]\n"
  "v_mfma_f32_16x16x32_bf16 v[2:5], v[10:13], v[30:33], v[2:5]\n"
  "v_max_f32_dpp v48, v48, v48 quad_perm:[2,3,0,1] row_mask:0xf bank_mask:0xf\n"
  "v_mov_b32 v49, v48\n"
  "s_nop 1\n"
  "v_permlane16_swap_b32 v48, v49\n"
  "v_max_f32 v48, v48, v49\n"
  "s_waitcnt lgkmcnt(5)\n"
  "ds_read_b128 v[10:13], v50 offset:256\n"
  "ds_read_b64_tr_b16 v[26:27], v51 offset:20480\n"
  "ds_read_b64_tr_b16 v[28:29], v51 offset:21760\n"
  "ds_read_b64_tr_b16 v[30:31], v51 offset:20512\n"
  "ds_read_b64_tr_b16 v[32:33], v51 offset:21792\n"
  "v_mfma_f32_16x16x32_bf16 v[42:45], v[14:17], v[34:37], v[42:45]\n"
  "v_mfma_f32_16x16x32_bf16 v[2:5], v[14:17], v[38:41], v[2:5]\n"
  "v_sub_f32 v46, v46, v48\n"
  "v_exp_f32 v46, v46\n"
  "s_nop 0\n"
  "v_mul_f32 v47, v46, v47\n"
  "s_waitcnt lgkmcnt(5)\n"
  "ds_read_b128 v[14:17], v50 offset:320\n"
  "ds_read_b64_tr_b16 v[34:35], v51 offset:25600\n"
  "ds_read_b64_tr_b16 v[36:37], v51 offset:26880\n"
  "ds_read_b64_tr_b16 v[38:39], v51 offset:25632\n"
  "ds_read_b64_tr_b16 v[40:41], v51 offset:26912\n"
  "v_mfma_f32_16x16x32_bf16 v[42:45], v[6:9], v[18:21], v[42:45]\n"
  "v_mfma_f32_16x16x32_bf16 v[2:5], v[6:9], v[22:25], v[2:5]\n"
  "v_add_f32_dpp v47, v47, v47 quad_perm:[1,0,3,2] row_mask:0xf bank_mask:0xf\n"
  "s_nop 1\n"
  "v_add_f32_dpp v47, v47, v47 quad_perm:[2,3,0,1] row_mask:0xf bank_mask:0xf\n"
  "s_waitcnt lgkmcnt(5)\n"
  "v_mfma_f32_16x16x32_bf16 v[42:45], v[10:13], v[26:29], v[42:45]\n"
  "v_mfma_f32_16x16x32_bf16 v[2:5], v[10:13], v[30:33], v[2:5]\n"
  "v_mov_b32 v49, v47\n"
  "s_nop 1\n"
  "v_permlane16_swap_b32 v47, v49\n"
  "v_add_f32 v47, v47, v49\n"
  "v_rcp_f32 v47, v47\n"
  "s_waitcnt lgkmcnt(0)\n"
  "v_mfma_f32_16x16x32_bf16 v[42:45], v[14:17], v[34:37], v[42:45]\n"
  "v_mfma_f32_16x16x32_bf16 v[2:5], v[14:17], v[38:41], v[2:5]\n"
  "v_mul_f32 v46, v46, v47\n"
  "v_mov_b32 v9, s62\n"
  "s_cmp_eq_u32 s33, 2\n"
  "s_mov_b32 s71, 0x1041\n"
  "s_cselect_b32 s70, s71, 0x10411041\n"
  "s_nop 1\n"
  "v_mul_f32_dpp v6, v46, v42 quad_perm:[0,0,0,0] row_mask:0xf bank_mask:0xf\n"
  "v_mul_f32_dpp v7, v46, v2 quad_perm:[0,0,0,0] row_mask:0xf bank_mask:0xf\n"
  "s_nop 0\n"
  "v_fmac_f32_dpp v6, v46, v43 quad_perm:[1,1,1,1] row_mask:0xf bank_mask:0xf\n"
  "v_fmac_f32_dpp v7, v46, v3 quad_perm:[1,1,1,1] row_mask:0xf bank_mask:0xf\n"
  "s_nop 0\n"
  "v_fmac_f32_dpp v6, v46, v44 quad_perm:[2,2,2,2] row_mask:0xf bank_mask:0xf\n"
  "v_fmac_f32_dpp v7, v46, v4 quad_perm:[2,2,2,2] row_mask:0xf bank_mask:0xf\n"
  "s_nop 0\n"
  "v_fmac_f32_dpp v6, v46, v45 quad_perm:[3,3,3,3] row_mask:0xf bank_mask:0xf\n"
  "v_fmac_f32_dpp v7, v46, v5 quad_perm:[3,3,3,3] row_mask:0xf bank_mask:0xf\n"
  "s_nop 1\n"
  "v_permlane16_swap_b32 v6, v7\n"
  "v_add_f32 v6, v6, v7\n"
  "s_nop 1\n"
  "v_mov_b32_dpp v7, v6 row_shl:1 row_mask:0xf bank_mask:0xf bound_ctrl:0\n"
  "v_cvt_pk_bf16_f32 v6, v6, v7\n"
  "s_nop 1\n"
  "v_mov_b32_dpp v7, v6 row_shl:2 row_mask:0xf bank_mask:0xf bound_ctrl:0\n"
  "v_mov_b32_dpp v8, v6 row_shl:4 row_mask:0xf bank_mask:0xf bound_ctrl:0\n"
  "s_mov_b32 exec_lo, s70\n"
  "s_mov_b32 exec_hi, 0\n"
  "buffer_store_dwordx4 v[6:9], v53, s[56:59], s76 offen sc1\n"
  "s_mov_b64 exec, -1\n"
  "Lsk_7:\n"
  "s_add_u32 s70, s84, 0\n"
  "v_add_u32 v24, s70, v0\n"
  "v_mul_u32_u24 v25, 2731, v24\n"
  "v_lshrrev_b32 v25, 16, v25\n"
  "v_mul_u32_u24 v26, 24, v25\n"
  "v_sub_u32 v24, v24, v26\n"
  "v_and_b32 v26, 6, v25\n"
  "v_xor_b32 v24, v24, v26\n"
  "v_mul_u32_u24 v26, 12288, v25\n"
  "v_lshl_add_u32 v26, v24, 4, v26\n"
  "s_add_u32 s70, s84, 64\n"
  "v_add_u32 v24, s70, v0\n"
  "v_mul_u32_u24 v25, 2731, v24\n"
  "v_lshrrev_b32 v25, 16, v25\n"
  "v_mul_u32_u24 v27, 24, v25\n"
  "v_sub_u32 v24, v24, v27\n"
  "v_and_b32 v27, 6, v25\n"
  "v_xor_b32 v24, v24, v27\n"
  "v_mul_u32_u24 v27, 12288, v25\n"
  "v_lshl_add_u32 v27, v24, 4, v27\n"
  "s_add_u32 s70, s84, 128\n"
  "v_add_u32 v24, s70, v0\n"
  "v_mul_u32_u24 v25, 2731, v24\n"
  "v_lshrrev_b32 v25, 16, v25\n"
  "v_mul_u32_u24 v28, 24, v25\n"
  "v_sub_u32 v24, v24, v28\n"
  "v_and_b32 v28, 6, v25\n"
  "v_xor_b32 v24, v24, v28\n"
  "v_mul_u32_u24 v28, 12288, v25\n"
  "v_lshl_add_u32 v28, v24, 4, v28\n"
  "s_lshl_b32 s70, s84, 4\n"
  "s_add_u32 m0, s70, 98304\n"
  "s_add_u32 s71, s81, 0\n"
  "buffer_load_dwordx4 v26, s[36:39], s71 offen nt lds\n"
  "s_lshl_b32 s70, s84, 4\n"
  "s_add_u32 m0, s70, 99328\n"
  "s_add_u32 s71, s81, 0\n"
  "buffer_load_dwordx4 v27, s[36:39], s71 offen nt lds\n"
  "s_lshl_b32 s70, s84, 4\n"
  "s_add_u32 m0, s70, 100352\n"
  "s_add_u32 s71, s81, 0\n"
  "buffer_load_dwordx4 v28, s[36:39], s71 offen nt lds\n"
  "s_lshl_b32 s70, s84, 4\n"
  "s_add_u32 m0, s70, 101376\n"
  "s_add_u32 s71, s81, 98304\n"
  "buffer_load_dwordx4 v26, s[36:39], s71 offen nt lds\n"
  "s_lshl_b32 s70, s84, 4\n"
  "s_add_u32 m0, s70, 102400\n"
  "s_add_u32 s71, s81, 98304\n"
  "buffer_load_dwordx4 v27, s[36:39], s71 offen nt lds\n"
  "s_lshl_b32 s70, s84, 4\n"
  "s_add_u32 m0, s70, 103424\n"
  "s_add_u32 s71, s81, 98304\n"
  "buffer_load_dwordx4 v28, s[36:39], s71 offen nt lds\n"
  "s_lshl_b32 s70, s84, 4\n"
  "s_add_u32 m0, s70, 104448\n"
  "s_add_u32 s71, s81, 196608\n"
  "buffer_load_dwordx4 v26, s[36:39], s71 offen nt lds\n"
  "s_lshl_b32 s70, s84, 4\n"
  "s_add_u32 m0, s70, 105472\n"
  "s_add_u32 s71, s81, 196608\n"
  "s_mov_b32 exec_lo, 0xffffffff\n"
  "s_mov_b32 exec_hi, 0x0\n"
  "buffer_load_dwordx4 v27, s[36:39], s71 offen nt lds\n"
  "s_mov_b64 exec, -1\n"
  "v_and_b32 v24, 8, v0\n"
  "v_lshlrev_b32 v24, 3, v24\n"
  "v_and_b32 v25, 0x30, v0\n"
  "v_add_u32 v24, v24, v25\n"
  "v_and_b32 v29, 7, v0\n"
  "v_add_u32 v29, 0, v29\n"
  "v_mul_u32_u24 v29, 12288, v29\n"
  "v_add_u32 v29, v29, v24\n"
  "v_and_b32 v30, 7, v0\n"
  "v_add_u32 v30, 8, v30\n"
  "v_mul_u32_u24 v30, 12288, v30\n"
  "v_add_u32 v30, v30, v24\n"
  "v_and_b32 v31, 7, v0\n"
  "v_add_u32 v31, 16, v31\n"
  "v_mul_u32_u24 v31, 12288, v31\n"
  "v_add_u32 v31, v31, v24\n"
  "buffer_load_dwordx4 a[192:195], v29, s[40:43], s85 offen offset:0 nt\n"
  "buffer_load_dwordx4 a[196:199], v29, s[40:43], s85 offen offset:128 nt\n"
  "buffer_load_dwordx4 a[200:203], v29, s[40:43], s85 offen offset:256 nt\n"
  "buffer_load_dwordx4 a[204:207], v30, s[40:43], s85 offen offset:0 nt\n"
  "buffer_load_dwordx4 a[208:211], v30, s[40:43], s85 offen offset:128 nt\n"
  "buffer_load_dwordx4 a[212:215], v30, s[40:43], s85 offen offset:256 nt\n"
  "s_lshl_b32 s70, s33, 2\n"
  "v_and_b32 v2, 3, v0\n"
  "v_add_u32 v2, s70, v2\n"
  "v_min_u32 v3, 14, v2\n"
  "v_sub_u32 v18, v3, v2\n"
  "v_add_u32 v18, v18, v0\n"
  "v_lshlrev_b32 v18, 4, v18\n"
  "s_lshl_b32 s70, s35, 2\n"
  "s_add_u32 s70, s70, s33\n"
  "s_lshl_b32 s70, s70, 11\n"
  "s_add_u32 s88, s70, 917504\n"
  "v_mul_u32_u24 v4, 86, v3\n"
  "v_lshrrev_b32 v4, 8, v4\n"
  "v_mul_u32_u24 v5, 3, v4\n"
  "v_sub_u32 v3, v3, v5\n"
  "v_mul_u32_u24 v3, 3, v3\n"
  "v_lshrrev_b32 v5, 4, v0\n"
  "v_add_u32 v3, v3, v5\n"
  "v_cmp_gt_u32 vcc, 8, v3\n"
  "v_cmp_gt_u32_e64 s[66:67], 15, v2\n"
  "s_nop 0\n"
  "s_and_b64 s[68:69], vcc, s[66:67]\n"
  "s_and_b32 s68, s68, 0x000f000f\n"
  "s_and_b32 s69, s69, 0x000f\n"
  "v_lshl_add_u32 v3, v4, 3, v3\n"
  "v_lshlrev_b32 v3, 2, v3\n"
  "v_add_u32 v19, 160320, v3\n"
  "v_mov_b32 v4, 160480\n"
  "v_cndmask_b32_e64 v19, v4, v19, s[68:69]\n"
  "ds_read_b32 v21, v19\n"
  "v_and_b32 v4, 1, v0\n"
  "v_lshlrev_b32 v4, 4, v4\n"
  "v_mov_b32 v5, 0x3f80\n"
  "v_lshlrev_b32 v4, v4, v5\n"
  "v_mov_b32 v17, 0\n"
  "s_mov_b32 s66, 0x30003\n"
  "s_mov_b32 s67, 0x30003\n"
  "v_cndmask_b32_e64 v14, 0, v4, s[66:67]\n"
  "s_mov_b32 s66, 0x300030\n"
  "s_mov_b32 s67, 0x300030\n"
  "v_cndmask_b32_e64 v15, 0, v4, s[66:67]\n"
  "s_mov_b32 s66, 0x3000300\n"
  "s_mov_b32 s67, 0x3000300\n"
  "v_cndmask_b32_e64 v16, 0, v4, s[66:67]\n"
  "s_waitcnt lgkmcnt(0)\n"
  "v_lshlrev_b32 v20, 16, v21\n"
  "v_and_b32 v21, 0xffff0000, v21\n"
  "v_lshrrev_b32 v3, 2, v0\n"
  "v_mul_u32_u24 v2, 384, v3\n"
  "v_and_b32 v3, 6, v3\n"
  "v_lshl_add_u32 v2, v3, 4, v2\n"
  "v_and_b32 v3, 3, v0\n"
  "v_lshl_add_u32 v2, v3, 3, v2\n"
  "v_add_u32 v2, 98304, v2\n"
  "s_mul_i32 s70, s33, 3\n"
  "s_add_u32 s70, s70, 0\n"
  "s_and_b32 s71, s70, 3\n"
  "s_lshl_b32 s71, s71, 5\n"
  "s_lshr_b32 s70, s70, 2\n"
  "s_lshl_b32 s70, s70, 7\n"
  "v_xor_b32 v46, s71, v2\n"
  "v_add_u32 v46, s70, v46\n"
  "s_mul_i32 s70, s33, 3\n"
  "s_add_u32 s70, s70, 1\n"
  "s_and_b32 s71, s70, 3\n"
  "s_lshl_b32 s71, s71, 5\n"
  "s_lshr_b32 s70, s70, 2\n"
  "s_lshl_b32 s70, s70, 7\n"
  "v_xor_b32 v47, s71, v2\n"
  "v_add_u32 v47, s70, v47\n"
  "s_mul_i32 s70, s33, 3\n"
  "s_add_u32 s70, s70, 2\n"
  "s_and_b32 s71, s70, 3\n"
  "s_lshl_b32 s71, s71, 5\n"
  "s_lshr_b32 s70, s70, 2\n"
  "s_lshl_b32 s70, s70, 7\n"
  "v_xor_b32 v48, s71, v2\n"
  "v_add_u32 v48, s70, v48\n"
  "s_mov_b32 s66, 0x02100210\n"
  "s_mov_b32 s67, 0x02100210\n"
  "v_lshrrev_b32 v2, 4, v0\n"
  "v_lshlrev_b32 v2, 3, v2\n"
  "v_add_u32 v22, 160320, v2\n"
  "v_add_u32 v2, 160128, v2\n"
  "v_cndmask_b32_e64 v22, v22, v2, s[66:67]\n"
  "s_lshl_b32 s70, s34, 5\n"
  "s_lshl_b32 s71, s33, 3\n"
  "s_add_u32 s70, s70, s71\n"
  "s_add_u32 s70, s70, s35\n"
  "s_lshl_b32 s71, s70, 4\n"
  "s_lshl_b32 s70, s70, 8\n"
  "s_add_u32 s71, s71, 589824\n"
  "v_lshlrev_b32 v54, 4, v0\n"
  "v_add_u32 v54, s70, v54\n"
  "v_add_u32 v2, 327424, v54\n"
  "v_add_u32 v54, 65536, v54\n"
  "s_mov_b32 s66, 0xffff0000\n"
  "s_mov_b32 s67, 0\n"
  "v_cndmask_b32_e64 v54, v54, v2, s[66:67]\n"
  "v_mov_b32 v2, s71\n"
  "s_mov_b32 s66, 0\n"
  "s_mov_b32 s67, -1\n"
  "v_cndmask_b32_e64 v54, v54, v2, s[66:67]\n"
  "LCA3:\n"
  "s_lshl_b32 s99, s33, 3\n"
  "s_lshr_b32 s98, 0x74393939, s99\n"
  "s_and_b32 s98, s98, 0xff\n"
  "Ldl_8:\n"
  "s_sub_u32 s98, s98, 1\n"
  "s_cbranch_scc0 Ldl_8\n"
  "buffer_load_dwordx4 v[6:9], v18, s[56:59], s88 offen offset:0 sc1\n"
  "buffer_load_dwordx4 v[10:13], v18, s[56:59], s88 offen offset:1024 sc1\n"
  "LPA3_9:\n"
  "s_waitcnt vmcnt(1)\n"
  "v_mfma_f32_16x16x32_bf16 v[2:5], v[14:17], v[6:9], 0\n"
  "v_cmp_ne_u32_e64 s[64:65], s62, v9\n"
  "s_waitcnt vmcnt(0)\n"
  "v_mfma_f32_16x16x32_bf16 v[2:5], v[14:17], v[10:13], v[2:5]\n"
  "v_cmp_ne_u32 vcc, s62, v13\n"
  "s_nop 0\n"
  "s_or_b64 s[64:65], s[64:65], vcc\n"
  "s_cbranch_scc1 LSA3_10\n"
  "s_nop 3\n"
  "v_add_f32_dpp v2, v2, v2 row_ror:4 row_mask:0xf bank_mask:0xf\n"
  "v_add_f32_dpp v3, v3, v3 row_ror:4 row_mask:0xf bank_mask:0xf\n"
  "s_nop 0\n"
  "v_add_f32_dpp v2, v2, v2 row_ror:8 row_mask:0xf bank_mask:0xf\n"
  "v_add_f32_dpp v3, v3, v3 row_ror:8 row_mask:0xf bank_mask:0xf\n"
  "v_cvt_pk_bf16_f32 v3, v2, v3\n"
  "v_lshlrev_b32 v2, 16, v3\n"
  "v_and_b32 v3, 0xffff0000, v3\n"
  "v_add_f32 v2, v20, v2\n"
  "v_add_f32 v3, v21, v3\n"
  "v_cvt_pk_bf16_f32 v2, v2, v3\n"
  "ds_write_b32 v19, v2\n"
  "s_add_u32 s62, s62, 1\n"
  "s_waitcnt lgkmcnt(0)\n"
  "s_barrier\n"
  "ds_read2_b64 v[6:9], v22 offset0:0 offset1:4\n"
  "ds_read2_b64 v[10:13], v22 offset0:8 offset1:12\n"
  "ds_read_b64 v[4:5], v22 offset:128\n"
  "v_mov_b32 v2, 0\n"
  "v_mov_b32 v3, 0\n"
  "s_mov_b32 s66, 0\n"
  "s_mov_b32 s67, -1\n"
  "ds_read_b64_tr_b16 v[16:17], v46 offset:0\n"
  "ds_read_b64_tr_b16 v[18:19], v46 offset:6144\n"
  "ds_read_b64_tr_b16 v[20:21], v46 offset:12288\n"
  "ds_read_b64_tr_b16 v[22:23], v46 offset:18432\n"
  "ds_read_b64_tr_b16 v[24:25], v46 offset:24576\n"
  "ds_read_b64_tr_b16 v[26:27], v47 offset:0\n"
  "ds_read_b64_tr_b16 v[28:29], v47 offset:6144\n"
  "ds_read_b64_tr_b16 v[30:31], v47 offset:12288\n"
  "ds_read_b64_tr_b16 v[32:33], v47 offset:18432\n"
  "ds_read_b64_tr_b16 v[34:35], v47 offset:24576\n"
  "s_waitcnt lgkmcnt(10)\n"
  "v_mfma_f32_16x16x32_bf16 v[50:53], v[6:9], v[6:9], 0\n"
  "v_mfma_f32_16x16x32_bf16 v[50:53], v[10:13], v[10:13], v[50:53]\n"
  "v_mfma_f32_16x16x32_bf16 v[50:53], v[2:5], v[2:5], v[50:53]\n"
  "ds_read_b64_tr_b16 v[36:37], v48 offset:0\n"
  "ds_read_b64_tr_b16 v[38:39], v48 offset:6144\n"
  "ds_read_b64_tr_b16 v[40:41], v48 offset:12288\n"
  "ds_read_b64_tr_b16 v[42:43], v48 offset:18432\n"
  "ds_read_b64_tr_b16 v[44:45], v48 offset:24576\n"
  "s_waitcnt lgkmcnt(10)\n"
  "v_mfma_f32_16x16x32_bf16 v[46:49], v[6:9], v[16:19], 0\n"
  "v_mfma_f32_16x16x32_bf16 v[46:49], v[10:13], v[20:23], v[46:49]\n"
  "v_mfma_f32_16x16x32_bf16 v[46:49], v[2:5], v[22:25], v[46:49]\n"
  "s_waitcnt lgkmcnt(5)\n"
  "v_mfma_f32_16x16x32_bf16 v[16:19], v[6:9], v[26:29], 0\n"
  "v_mfma_f32_16x16x32_bf16 v[16:19], v[10:13], v[30:33], v[16:19]\n"
  "v_mfma_f32_16x16x32_bf16 v[16:19], v[2:5], v[32:35], v[16:19]\n"
  "s_waitcnt lgkmcnt(0)\n"
  "v_mfma_f32_16x16x32_bf16 v[22:25], v[6:9], v[36:39], 0\n"
  "v_mfma_f32_16x16x32_bf16 v[22:25], v[10:13], v[40:43], v[22:25]\n"
  "v_mfma_f32_16x16x32_bf16 v[22:25], v[2:5], v[42:45], v[22:25]\n"
  "v_cndmask_b32_e64 v20, v46, v50, s[66:67]\n"
  "s_nop 2\n"
  "v_cndmask_b32_e64 v21, v16, v51, s[66:67]\n"
  "s_nop 2\n"
  "v_mov_b32 v23, s62\n"
  "s_mov_b32 exec_lo, 0xffffffff\n"
  "s_mov_b32 exec_hi, 0x1\n"
  "buffer_store_dwordx4 v[20:23], v54, s[56:59], 0 offen sc1\n"
  "s_mov_b64 exec, -1\n"
  "v_and_b32 v24, 8, v0\n"
  "v_lshlrev_b32 v24, 3, v24\n"
  "v_and_b32 v25, 0x30, v0\n"
  "v_add_u32 v24, v24, v25\n"
  "v_and_b32 v26, 7, v0\n"
  "v_add_u32 v26, 0, v26\n"
  "v_mul_u32_u24 v26, 12288, v26\n"
  "v_add_u32 v26, v26, v24\n"
  "v_and_b32 v27, 7, v0\n"
  "v_add_u32 v27, 8, v27\n"
  "v_mul_u32_u24 v27, 12288, v27\n"
  "v_add_u32 v27, v27, v24\n"
  "v_and_b32 v28, 7, v0\n"
  "v_add_u32 v28, 16, v28\n"
  "v_mul_u32_u24 v28, 12288, v28\n"
  "v_add_u32 v28, v28, v24\n"
  "buffer_load_dwordx4 a[216:219], v28, s[40:43], s85 offen offset:0 nt\n"
  "buffer_load_dwordx4 a[220:223], v28, s[40:43], s85 offen offset:128 nt\n"
  "buffer_load_dwordx4 v[216:219], v28, s[40:43], s85 offen offset:256 nt\n"
  "v_add_u32 v24, 0, v0\n"
  "v_mul_u32_u24 v25, 2731, v24\n"
  "v_lshrrev_b32 v25, 16, v25\n"
  "v_mul_u32_u24 v29, 24, v25\n"
  "v_sub_u32 v24, v24, v29\n"
  "v_and_b32 v29, 6, v25\n"
  "v_xor_b32 v24, v24, v29\n"
  "v_mul_u32_u24 v29, 1536, v25\n"
  "v_lshl_add_u32 v29, v24, 4, v29\n"
  "v_add_u32 v24, 64, v0\n"
  "v_mul_u32_u24 v25, 2731, v24\n"
  "v_lshrrev_b32 v25, 16, v25\n"
  "v_mul_u32_u24 v30, 24, v25\n"
  "v_sub_u32 v24, v24, v30\n"
  "v_and_b32 v30, 6, v25\n"
  "v_xor_b32 v24, v24, v30\n"
  "v_mul_u32_u24 v30, 1536, v25\n"
  "v_lshl_add_u32 v30, v24, 4, v30\n"
  "v_add_u32 v24, 128, v0\n"
  "v_mul_u32_u24 v25, 2731, v24\n"
  "v_lshrrev_b32 v25, 16, v25\n"
  "v_mul_u32_u24 v31, 24, v25\n"
  "v_sub_u32 v24, v24, v31\n"
  "v_and_b32 v31, 6, v25\n"
  "v_xor_b32 v24, v24, v31\n"
  "v_mul_u32_u24 v31, 1536, v25\n"
  "v_lshl_add_u32 v31, v24, 4, v31\n"
  "s_mul_i32 s72, s61, 98304\n"
  "s_mul_i32 s73, s33, 384\n"
  "s_add_u32 s72, s72, s73\n"
  "s_mul_i32 s73, s33, 24576\n"
  "s_add_u32 s71, s72, 0\n"
  "s_add_u32 m0, s73, 0\n"
  "s_nop 0\n"
  "buffer_load_dwordx4 v29, s[28:31], s71 offen  lds\n"
  "s_add_u32 m0, s73, 1024\n"
  "s_nop 0\n"
  "buffer_load_dwordx4 v30, s[28:31], s71 offen  lds\n"
  "s_add_u32 m0, s73, 2048\n"
  "s_nop 0\n"
  "buffer_load_dwordx4 v31, s[28:31], s71 offen  lds\n"
  "s_add_u32 s71, s72, 12288\n"
  "s_add_u32 m0, s73, 3072\n"
  "s_nop 0\n"
  "buffer_load_dwordx4 v29, s[28:31], s71 offen  lds\n"
  "s_add_u32 m0, s73, 4096\n"
  "s_nop 0\n"
  "buffer_load_dwordx4 v30, s[28:31], s71 offen  lds\n"
  "s_add_u32 m0, s73, 5120\n"
  "s_nop 0\n"
  "buffer_load_dwordx4 v31, s[28:31], s71 offen  lds\n"
  "s_add_u32 s71, s72, 24576\n"
  "s_add_u32 m0, s73, 6144\n"
  "s_nop 0\n"
  "buffer_load_dwordx4 v29, s[28:31], s71 offen  lds\n"
  "s_add_u32 m0, s73, 7168\n"
  "s_nop 0\n"
  "buffer_load_dwordx4 v30, s[28:31], s71 offen  lds\n"
  "s_add_u32 m0, s73, 8192\n"
  "s_nop 0\n"
  "buffer_load_dwordx4 v31, s[28:31], s71 offen  lds\n"
  "s_add_u32 s71, s72, 36864\n"
  "s_add_u32 m0, s73, 9216\n"
  "s_nop 0\n"
  "buffer_load_dwordx4 v29, s[28:31], s71 offen  lds\n"
  "v_lshlrev_b32 v26, 4, v0\n"
  "v_lshrrev_b32 v27, 1, v0\n"
  "v_and_b32 v27, 7, v27\n"
  "v_lshlrev_b32 v27, 4, v27\n"
  "s_lshl_b32 s70, s34, 2\n"
  "s_add_u32 s70, s70, s33\n"
  "s_lshl_b32 s71, s70, 7\n"
  "s_lshl_b32 s70, s70, 11\n"
  "s_add_u32 s88, s71, 589824\n"
  "s_add_u32 s89, s70, 65536\n"
  "s_add_u32 s90, s70, 327680\n"
  "v_lshrrev_b32 v28, 4, v0\n"
  "v_lshlrev_b32 v28, 1, v28\n"
  "v_mov_b32 v29, 0x98\n"
  "v_lshrrev_b32 v28, v28, v29\n"
  "v_and_b32 v28, 3, v28\n"
  "v_and_b32 v29, 15, v0\n"
  "v_lshl_add_u32 v28, v28, 4, v29\n"
  "s_mul_i32 s70, s33, 48\n"
  "v_add_u32 v28, s70, v28\n"
  "v_lshlrev_b32 v29, 2, v28\n"
  "v_add_u32 v29, 161664, v29\n"
  "v_lshlrev_b32 v28, 1, v28\n"
  "v_add_u32 v28, 160512, v28\n"
  "v_and_b32 v40, 1, v0\n"
  "v_lshlrev_b32 v40, 6, v40\n"
  "v_and_b32 v41, 0x30, v0\n"
  "v_add_u32 v40, v40, v41\n"
  "v_add_u32 v40, 160512, v40\n"
  "v_and_b32 v41, 1, v0\n"
  "v_lshlrev_b32 v41, 10, v41\n"
  "v_and_b32 v42, 8, v0\n"
  "v_lshl_add_u32 v41, v42, 8, v41\n"
  "v_and_b32 v42, 6, v0\n"
  "v_lshl_add_u32 v41, v42, 1, v41\n"
  "s_lshr_b32 s70, s34, 3\n"
  "s_lshl_b32 s70, s70, 2\n"
  "s_lshr_b32 s71, s35, 1\n"
  "s_add_u32 s70, s70, s71\n"
  "s_mul_i32 s76, s70, 3072\n"
  "s_and_b32 s70, s34, 4\n"
  "s_add_u32 s70, s70, s33\n"
  "s_lshl_b32 s70, s70, 1\n"
  "s_and_b32 s71, s35, 1\n"
  "s_add_u32 s70, s70, s71\n"
  "s_lshl_b32 s70, s70, 2\n"
  "s_and_b32 s71, s34, 3\n"
  "s_add_u32 s70, s70, s71\n"
  "s_lshl_b32 s70, s70, 4\n"
  "s_add_u32 s70, s70, 720896\n"
  "s_add_u32 s76, s76, s70\n"
  "LCB1:\n"
  "s_lshl_b32 s99, s33, 3\n"
  "s_lshr_b32 s98, 0x9090909, s99\n"
  "s_and_b32 s98, s98, 0xff\n"
  "Ldl_11:\n"
  "s_sub_u32 s98, s98, 1\n"
  "s_cbranch_scc0 Ldl_11\n"
  "buffer_load_dwordx4 v[6:9], v27, s[56:59], s88 offen sc1\n"
  "buffer_load_dwordx4 v[18:21], v26, s[56:59], s90 offen sc1\n"
  "buffer_load_dwordx4 v[22:25], v26, s[56:59], s90 offen offset:1024 sc1\n"
  "buffer_load_dwordx4 v[10:13], v26, s[56:59], s89 offen sc1\n"
  "buffer_load_dwordx4 v[14:17], v26, s[56:59], s89 offen offset:1024 sc1\n"
  "LPB1_12:\n"
  "s_waitcnt vmcnt(4)\n"
  "v_cmp_ne_u32_e64 s[64:65], s62, v9\n"
  "v_add_f32_dpp v6, v6, v6 quad_perm:[2,3,0,1] row_mask:0xf bank_mask:0xf\n"
  "v_add_f32_dpp v7, v7, v7 quad_perm:[2,3,0,1] row_mask:0xf bank_mask:0xf\n"
  "s_nop 0\n"
  "v_add_f32_dpp v6, v6, v6 row_ror:4 row_mask:0xf bank_mask:0xf\n"
  "v_add_f32_dpp v7, v7, v7 row_ror:4 row_mask:0xf bank_mask:0xf\n"
  "s_nop 0\n"
  "v_add_f32_dpp v6, v6, v6 row_ror:8 row_mask:0xf bank_mask:0xf\n"
  "v_add_f32_dpp v7, v7, v7 row_ror:8 row_mask:0xf bank_mask:0xf\n"
  "v_mul_f32 v7, 0x3acccccd, v7\n"
  "v_mul_f32 v8, v7, v7\n"
  "v_mov_b32 v30, 0x3acccccd\n"
  "v_fma_f32 v6, v6, v30, -v8\n"
  "v_max_f32 v6, 0, v6\n"
  "v_add_f32 v6, 0x3727c5ac, v6\n"
  "v_rsq_f32 v6, v6\n"
  "s_waitcnt vmcnt(2)\n"
  "v_cmp_ne_u32 vcc, s62, v21\n"
  "s_nop 0\n"
  "s_or_b64 s[64:65], s[64:65], vcc\n"
  "v_cmp_ne_u32 vcc, s62, v25\n"
  "s_nop 0\n"
  "s_or_b64 s[64:65], s[64:65], vcc\n"
  "v_add_f32 v18, v18, v22\n"
  "v_add_f32 v19, v19, v23\n"
  "v_add_f32 v20, v20, v24\n"
  "v_mov_b32 v30, v20\n"
  "v_permlane32_swap_b32 v18, v19\n"
  "s_nop 0\n"
  "v_permlane32_swap_b32 v20, v30\n"
  "v_add_f32 v18, v18, v19\n"
  "v_add_f32 v20, v20, v30\n"
  "s_nop 1\n"
  "v_permlane16_swap_b32 v18, v20\n"
  "v_add_f32 v18, v18, v20\n"
  "v_mul_f32 v7, v7, v18\n"
  "s_waitcnt vmcnt(1)\n"
  "v_cmp_ne_u32 vcc, s62, v13\n"
  "s_nop 0\n"
  "s_or_b64 s[64:65], s[64:65], vcc\n"
  "s_waitcnt vmcnt(0)\n"
  "v_cmp_ne_u32 vcc, s62, v17\n"
  "s_nop 0\n"
  "s_or_b64 s[64:65], s[64:65], vcc\n"
  "s_cbranch_scc1 LSB1_13\n"
  "v_add_f32 v10, v10, v14\n"
  "v_add_f32 v11, v11, v15\n"
  "v_add_f32 v12, v12, v16\n"
  "v_mov_b32 v30, v12\n"
  "v_permlane32_swap_b32 v10, v11\n"
  "s_nop 0\n"
  "v_permlane32_swap_b32 v12, v30\n"
  "v_add_f32 v10, v10, v11\n"
  "v_add_f32 v12, v12, v30\n"
  "s_nop 1\n"
  "v_permlane16_swap_b32 v10, v12\n"
  "v_add_f32 v10, v10, v12\n"
  "v_sub_f32 v10, v10, v7\n"
  "v_mul_f32 v10, v10, v6\n"
  "v_cvt_pk_bf16_f32 v10, v10, v10\n"
  "ds_write_b16 v28, v10\n"
  "ds_write_b32 v29, v18\n"
  "s_waitcnt lgkmcnt(0)\n"
  "s_barrier\n"
  "ds_read_b128 v[6:9], v40 offset:0\n"
  "ds_read_b128 v[10:13], v40 offset:128\n"
  "ds_read_b128 v[14:17], v40 offset:256\n"
  "s_waitcnt lgkmcnt(2)\n"
  "v_mfma_f32_16x16x32_bf16 v[18:21], v[6:9], a[192:195], 0\n"
  "v_mfma_f32_16x16x32_bf16 v[22:25], v[6:9], a[204:207], 0\n"
  "v_mfma_f32_16x16x32_bf16 v[26:29], v[6:9], a[216:219], 0\n"
  "s_waitcnt lgkmcnt(1)\n"
  "v_mfma_f32_16x16x32_bf16 v[18:21], v[10:13], a[196:199], v[18:21]\n"
  "v_mfma_f32_16x16x32_bf16 v[22:25], v[10:13], a[208:211], v[22:25]\n"
  "v_mfma_f32_16x16x32_bf16 v[26:29], v[10:13], a[220:223], v[26:29]\n"
  "s_waitcnt lgkmcnt(0)\n"
  "v_mfma_f32_16x16x32_bf16 v[18:21], v[14:17], a[200:203], v[18:21]\n"
  "v_mfma_f32_16x16x32_bf16 v[22:25], v[14:17], a[212:215], v[22:25]\n"
  "v_mfma_f32_16x16x32_bf16 v[26:29], v[14:17], v[216:219], v[26:29]\n"
  "s_mov_b32 vcc_lo, 0xaaaaaaaa\n"
  "s_mov_b32 vcc_hi, 0xaaaaaaaa\n"
  "s_nop 3\n"
  "v_add_f32_dpp v18, v19, v18 row_ror:8 row_mask:0xf bank_mask:0xf\n"
  "v_add_f32_dpp v22, v23, v22 row_ror:8 row_mask:0xf bank_mask:0xf\n"
  "s_nop 0\n"
  "v_add_f32_dpp v18, v26, v27 row_ror:8 row_mask:0xf bank_mask:0xc\n"
  "v_mov_b32_dpp v23, v22 quad_perm:[1,0,3,2] row_mask:0xf bank_mask:0xf\n"
  "s_nop 0\n"
  "v_mov_b32_dpp v19, v18 quad_perm:[1,0,3,2] row_mask:0xf bank_mask:0xf\n"
  "v_cvt_pk_bf16_f32 v31, v23, v22\n"
  "v_cvt_pk_bf16_f32 v30, v18, v19\n"
  "v_cndmask_b32 v30, v30, v31, vcc\n"
  "s_mov_b32 exec_lo, 0x55ff\n"
  "s_mov_b32 exec_hi, 0x0\n"
  "buffer_store_dword v30, v41, s[56:59], s76 offen sc1\n"
  "s_mov_b64 exec, -1\n"
  "v_add_u32 v24, 0, v0\n"
  "v_mul_u32_u24 v25, 2731, v24\n"
  "v_lshrrev_b32 v25, 16, v25\n"
  "v_mul_u32_u24 v26, 24, v25\n"
  "v_sub_u32 v24, v24, v26\n"
  "v_and_b32 v26, 6, v25\n"
  "v_xor_b32 v24, v24, v26\n"
  "v_mul_u32_u24 v26, 1536, v25\n"
  "v_lshl_add_u32 v26, v24, 4, v26\n"
  "v_add_u32 v24, 64, v0\n"
  "v_mul_u32_u24 v25, 2731, v24\n"
  "v_lshrrev_b32 v25, 16, v25\n"
  "v_mul_u32_u24 v27, 24, v25\n"
  "v_sub_u32 v24, v24, v27\n"
  "v_and_b32 v27, 6, v25\n"
  "v_xor_b32 v24, v24, v27\n"
  "v_mul_u32_u24 v27, 1536, v25\n"
  "v_lshl_add_u32 v27, v24, 4, v27\n"
  "v_add_u32 v24, 128, v0\n"
  "v_mul_u32_u24 v25, 2731, v24\n"
  "v_lshrrev_b32 v25, 16, v25\n"
  "v_mul_u32_u24 v28, 24, v25\n"
  "v_sub_u32 v24, v24, v28\n"
  "v_and_b32 v28, 6, v25\n"
  "v_xor_b32 v24, v24, v28\n"
  "v_mul_u32_u24 v28, 1536, v25\n"
  "v_lshl_add_u32 v28, v24, 4, v28\n"
  "s_mul_i32 s72, s61, 98304\n"
  "s_mul_i32 s73, s33, 384\n"
  "s_add_u32 s72, s72, s73\n"
  "s_mul_i32 s73, s33, 24576\n"
  "s_add_u32 s71, s72, 36864\n"
  "s_add_u32 m0, s73, 10240\n"
  "s_nop 0\n"
  "buffer_load_dwordx4 v27, s[28:31], s71 offen  lds\n"
  "s_add_u32 m0, s73, 11264\n"
  "s_nop 0\n"
  "buffer_load_dwordx4 v28, s[28:31], s71 offen  lds\n"
  "s_add_u32 s71, s72, 49152\n"
  "s_add_u32 m0, s73, 12288\n"
  "s_nop 0\n"
  "buffer_load_dwordx4 v26, s[28:31], s71 offen  lds\n"
  "s_add_u32 m0, s73, 13312\n"
  "s_nop 0\n"
  "buffer_load_dwordx4 v27, s[28:31], s71 offen  lds\n"
  "s_add_u32 m0, s73, 14336\n"
  "s_nop 0\n"
  "buffer_load_dwordx4 v28, s[28:31], s71 offen  lds\n"
  "s_add_u32 s71, s72, 61440\n"
  "s_add_u32 m0, s73, 15360\n"
  "s_nop 0\n"
  "buffer_load_dwordx4 v26, s[28:31], s71 offen  lds\n"
  "s_add_u32 m0, s73, 16384\n"
  "s_nop 0\n"
  "buffer_load_dwordx4 v27, s[28:31], s71 offen  lds\n"
  "s_add_u32 m0, s73, 17408\n"
  "s_nop 0\n"
  "buffer_load_dwordx4 v28, s[28:31], s71 offen  lds\n"
  "s_add_u32 s71, s72, 73728\n"
  "s_add_u32 m0, s73, 18432\n"
  "s_nop 0\n"
  "buffer_load_dwordx4 v26, s[28:31], s71 offen  lds\n"
  "s_add_u32 m0, s73, 19456\n"
  "s_nop 0\n"
  "buffer_load_dwordx4 v27, s[28:31], s71 offen  lds\n"
  "s_add_u32 m0, s73, 20480\n"
  "s_nop 0\n"
  "buffer_load_dwordx4 v28, s[28:31], s71 offen  lds\n"
  "s_add_u32 s71, s72, 86016\n"
  "s_add_u32 m0, s73, 21504\n"
  "s_nop 0\n"
  "buffer_load_dwordx4 v26, s[28:31], s71 offen  lds\n"
  "s_add_u32 m0, s73, 22528\n"
  "s_nop 0\n"
  "buffer_load_dwordx4 v27, s[28:31], s71 offen  lds\n"
  "s_add_u32 m0, s73, 23552\n"
  "s_nop 0\n"
  "buffer_load_dwordx4 v28, s[28:31], s71 offen  lds\n"
  "v_and_b32 v1, 3, v0\n"
  "v_lshlrev_b32 v1, 4, v1\n"
  "v_and_b32 v54, 0x38, v0\n"
  "v_lshl_add_u32 v1, v54, 3, v1\n"
  "v_and_b32 v54, 4, v0\n"
  "v_lshl_add_u32 v1, v54, 7, v1\n"
  "s_lshl_b32 s70, s60, 2\n"
  "s_add_u32 s70, s70, s33\n"
  "s_mul_i32 s70, s70, 3072\n"
  "s_add_u32 s88, s70, 720896\n"
  "s_lshl_b32 s70, s60, 5\n"
  "s_add_u32 s70, s70, s33\n"
  "s_lshl_b32 s70, s70, 6\n"
  "s_add_u32 s70, s70, s61\n"
  "s_mul_i32 s70, s70, 512\n"
  "s_add_u32 s93, s70, 4194304\n"
  "s_lshl_b32 s70, s60, 9\n"
  "s_add_u32 s70, s70, s61\n"
  "s_lshl_b32 s70, s70, 4\n"
  "s_add_u32 s94, s70, 851968\n"
  "s_mul_i32 s75, s33, 24576\n"
  "v_and_b32 v52, 15, v0\n"
  "v_mul_u32_u24 v52, 384, v52\n"
  "v_add_u32 v52, s75, v52\n"
  "v_lshrrev_b32 v53, 4, v0\n"
  "v_mul_u32_u24 v53, 3, v53\n"
  "v_and_b32 v54, 6, v0\n"
  "v_add_u32 v46, 0, v53\n"
  "v_xor_b32 v46, v46, v54\n"
  "v_lshl_add_u32 v46, v46, 4, v52\n"
  "v_add_u32 v47, 1, v53\n"
  "v_xor_b32 v47, v47, v54\n"
  "v_lshl_add_u32 v47, v47, 4, v52\n"
  "v_add_u32 v48, 2, v53\n"
  "v_xor_b32 v48, v48, v54\n"
  "v_lshl_add_u32 v48, v48, 4, v52\n"
  "v_add_u32 v49, 12, v53\n"
  "v_xor_b32 v49, v49, v54\n"
  "v_lshl_add_u32 v49, v49, 4, v52\n"
  "v_add_u32 v50, 13, v53\n"
  "v_xor_b32 v50, v50, v54\n"
  "v_lshl_add_u32 v50, v50, 4, v52\n"
  "v_add_u32 v51, 14, v53\n"
  "v_xor_b32 v51, v51, v54\n"
  "v_lshl_add_u32 v51, v51, 4, v52\n"
  "v_and_b32 v52, 7, v0\n"
  "v_mul_u32_u24 v52, 272, v52\n"
  "v_and_b32 v54, 0x30, v0\n"
  "v_add_u32 v52, v52, v54\n"
  "v_add_u32 v52, 134528, v52\n"
  "v_and_b32 v54, 8, v0\n"
  "v_lshl_add_u32 v53, v54, 4, v52\n"
  "s_mul_i32 s70, s33, 7680\n"
  "v_add_u32 v52, s70, v52\n"
  "s_add_u32 s70, s84, 0\n"
  "v_add_u32 v44, s70, v0\n"
  "v_mul_u32_u24 v45, 6554, v44\n"
  "v_lshrrev_b32 v45, 16, v45\n"
  "v_mul_u32_u24 v26, 10, v45\n"
  "v_sub_u32 v44, v44, v26\n"
  "v_lshrrev_b32 v26, 1, v45\n"
  "v_xor_b32 v26, v26, v45\n"
  "v_and_b32 v26, 4, v26\n"
  "v_mul_u32_u24 v26, 3, v26\n"
  "v_xor_b32 v45, v45, v26\n"
  "v_mul_u32_u24 v26, 1280, v45\n"
  "v_lshl_add_u32 v26, v44, 4, v26\n"
  "s_add_u32 s70, s84, 64\n"
  "v_add_u32 v44, s70, v0\n"
  "v_mul_u32_u24 v45, 6554, v44\n"
  "v_lshrrev_b32 v45, 16, v45\n"
  "v_mul_u32_u24 v27, 10, v45\n"
  "v_sub_u32 v44, v44, v27\n"
  "v_lshrrev_b32 v27, 1, v45\n"
  "v_xor_b32 v27, v27, v45\n"
  "v_and_b32 v27, 4, v27\n"
  "v_mul_u32_u24 v27, 3, v27\n"
  "v_xor_b32 v45, v45, v27\n"
  "v_mul_u32_u24 v27, 1280, v45\n"
  "v_lshl_add_u32 v27, v44, 4, v27\n"
  "s_add_u32 s70, s84, 128\n"
  "v_add_u32 v44, s70, v0\n"
  "v_mul_u32_u24 v45, 6554, v44\n"
  "v_lshrrev_b32 v45, 16, v45\n"
  "v_mul_u32_u24 v28, 10, v45\n"
  "v_sub_u32 v44, v44, v28\n"
  "v_lshrrev_b32 v28, 1, v45\n"
  "v_xor_b32 v28, v28, v45\n"
  "v_and_b32 v28, 4, v28\n"
  "v_mul_u32_u24 v28, 3, v28\n"
  "v_xor_b32 v45, v45, v28\n"
  "v_mul_u32_u24 v28, 1280, v45\n"
  "v_lshl_add_u32 v28, v44, 4, v28\n"
  "s_add_u32 s70, s84, 192\n"
  "v_add_u32 v44, s70, v0\n"
  "v_mul_u32_u24 v45, 6554, v44\n"
  "v_lshrrev_b32 v45, 16, v45\n"
  "v_mul_u32_u24 v29, 10, v45\n"
  "v_sub_u32 v44, v44, v29\n"
  "v_lshrrev_b32 v29, 1, v45\n"
  "v_xor_b32 v29, v29, v45\n"
  "v_and_b32 v29, 4, v29\n"
  "v_mul_u32_u24 v29, 3, v29\n"
  "v_xor_b32 v45, v45, v29\n"
  "v_mul_u32_u24 v29, 1280, v45\n"
  "v_lshl_add_u32 v29, v44, 4, v29\n"
  "s_add_u32 s70, s84, 256\n"
  "v_add_u32 v44, s70, v0\n"
  "v_mul_u32_u24 v45, 6554, v44\n"
  "v_lshrrev_b32 v45, 16, v45\n"
  "v_mul_u32_u24 v30, 10, v45\n"
  "v_sub_u32 v44, v44, v30\n"
  "v_lshrrev_b32 v30, 1, v45\n"
  "v_xor_b32 v30, v30, v45\n"
  "v_and_b32 v30, 4, v30\n"
  "v_mul_u32_u24 v30, 3, v30\n"
  "v_xor_b32 v45, v45, v30\n"
  "v_mul_u32_u24 v30, 1280, v45\n"
  "v_lshl_add_u32 v30, v44, 4, v30\n"
  "v_add_u32 v44, 0, v0\n"
  "v_mul_u32_u24 v45, 2731, v44\n"
  "v_lshrrev_b32 v45, 16, v45\n"
  "v_mul_u32_u24 v252, 24, v45\n"
  "v_sub_u32 v44, v44, v252\n"
  "v_and_b32 v252, 6, v45\n"
  "v_xor_b32 v44, v44, v252\n"
  "v_mul_u32_u24 v252, 12288, v45\n"
  "v_lshl_add_u32 v252, v44, 4, v252\n"
  "v_add_u32 v44, 64, v0\n"
  "v_mul_u32_u24 v45, 2731, v44\n"
  "v_lshrrev_b32 v45, 16, v45\n"
  "v_mul_u32_u24 v253, 24, v45\n"
  "v_sub_u32 v44, v44, v253\n"
  "v_and_b32 v253, 6, v45\n"
  "v_xor_b32 v44, v44, v253\n"
  "v_mul_u32_u24 v253, 12288, v45\n"
  "v_lshl_add_u32 v253, v44, 4, v253\n"
  "v_add_u32 v44, 128, v0\n"
  "v_mul_u32_u24 v45, 2731, v44\n"
  "v_lshrrev_b32 v45, 16, v45\n"
  "v_mul_u32_u24 v254, 24, v45\n"
  "v_sub_u32 v44, v44, v254\n"
  "v_and_b32 v254, 6, v45\n"
  "v_xor_b32 v44, v44, v254\n"
  "v_mul_u32_u24 v254, 12288, v45\n"
  "v_lshl_add_u32 v254, v44, 4, v254\n"
  "LCB2:\n"
  "s_lshl_b32 s99, s33, 3\n"
  "s_lshr_b32 s98, 0x10101010, s99\n"
  "s_and_b32 s98, s98, 0xff\n"
  "Ldl_14:\n"
  "s_sub_u32 s98, s98, 1\n"
  "s_cbranch_scc0 Ldl_14\n"
  "buffer_load_dwordx4 v[6:9], v1, s[56:59], s88 offen offset:0 sc1\n"
  "buffer_load_dwordx4 v[22:25], v1, s[56:59], s88 offen offset:1024 sc1\n"
  "buffer_load_dwordx4 v[14:17], v1, s[56:59], s88 offen offset:2048 sc1\n"
  "LPB2_15:\n"
  "s_waitcnt vmcnt(2)\n"
  "v_max3_u32 v10, v6, v6, v7\n"
  "v_max3_u32 v10, v10, v8, v9\n"
  "s_waitcnt vmcnt(1)\n"
  "v_max3_u32 v10, v10, v22, v23\n"
  "v_max3_u32 v10, v10, v24, v25\n"
  "s_waitcnt vmcnt(0)\n"
  "v_max3_u32 v10, v10, v14, v15\n"
  "v_max3_u32 v10, v10, v16, v17\n"
  "v_cmp_eq_u32 vcc, -1, v10\n"
  "s_nop 0\n"
  "s_or_b64 s[64:65], vcc, vcc\n"
  "s_cbranch_scc1 LSB2_16\n"
  "ds_read_b128 v[34:37], v46 offset:0\n"
  "ds_read_b128 v[38:41], v46 offset:6144\n"
  "ds_read_b128 v[42:45], v46 offset:12288\n"
  "ds_read_b128 v[2:5], v46 offset:18432\n"
  "s_lshl_b32 s70, s84, 4\n"
  "s_add_u32 m0, s70, 129024\n"
  "s_add_u32 s71, s82, 0\n"
  "buffer_load_dwordx4 v26, s[48:51], s71 offen nt lds\n"
  "s_add_u32 s72, s85, 0\n"
  "buffer_load_dwordx4 v[220:223], v252, s[44:47], s72 offen nt\n"
  "buffer_load_dwordx4 v[224:227], v253, s[44:47], s72 offen nt\n"
  "buffer_load_dwordx4 v[228:231], v254, s[44:47], s72 offen nt\n"
  "s_add_u32 s72, s85, 98304\n"
  "buffer_load_dwordx4 v[232:235], v252, s[44:47], s72 offen nt\n"
  "buffer_load_dwordx4 v[236:239], v253, s[44:47], s72 offen nt\n"
  "buffer_load_dwordx4 v[240:243], v254, s[44:47], s72 offen nt\n"
  "s_add_u32 s72, s85, 196608\n"
  "buffer_load_dwordx4 v[244:247], v252, s[44:47], s72 offen nt\n"
  "buffer_load_dwordx4 v[248:251], v253, s[44:47], s72 offen nt\n"
  "buffer_load_dwordx4 v[252:255], v254, s[44:47], s72 offen nt\n"
  "s_waitcnt lgkmcnt(3)\n"
  "v_mfma_f32_16x16x32_bf16 v[10:13], v[34:37], v[6:9], 0\n"
  "ds_read_b128 v[34:37], v47 offset:0\n"
  "s_waitcnt lgkmcnt(3)\n"
  "v_mfma_f32_16x16x32_bf16 v[18:21], v[38:41], v[6:9], 0\n"
  "ds_read_b128 v[38:41], v47 offset:6144\n"
  "s_waitcnt lgkmcnt(3)\n"
  "v_mfma_f32_16x16x32_bf16 v[26:29], v[42:45], v[6:9], 0\n"
  "ds_read_b128 v[42:45], v47 offset:12288\n"
  "s_waitcnt lgkmcnt(3)\n"
  "v_mfma_f32_16x16x32_bf16 v[30:33], v[2:5], v[6:9], 0\n"
  "ds_read_b128 v[2:5], v47 offset:18432\n"
  "s_waitcnt lgkmcnt(3)\n"
  "v_mfma_f32_16x16x32_bf16 v[10:13], v[34:37], v[22:25], v[10:13]\n"
  "ds_read_b128 v[34:37], v48 offset:0\n"
  "v_mov_b32_dpp v6, v6 row_ror:8 row_mask:0xf bank_mask:0xf\n"
  "s_waitcnt lgkmcnt(3)\n"
  "v_mfma_f32_16x16x32_bf16 v[18:21], v[38:41], v[22:25], v[18:21]\n"
  "ds_read_b128 v[38:41], v48 offset:6144\n"
  "v_mov_b32_dpp v7, v7 row_ror:8 row_mask:0xf bank_mask:0xf\n"
  "s_waitcnt lgkmcnt(3)\n"
  "v_mfma_f32_16x16x32_bf16 v[26:29], v[42:45], v[22:25], v[26:29]\n"
  "ds_read_b128 v[42:45], v48 offset:12288\n"
  "v_mov_b32_dpp v8, v8 row_ror:8 row_mask:0xf bank_mask:0xf\n"
  "s_waitcnt lgkmcnt(3)\n"
  "v_mfma_f32_16x16x32_bf16 v[30:33], v[2:5], v[22:25], v[30:33]\n"
  "ds_read_b128 v[2:5], v48 offset:18432\n"
  "v_mov_b32_dpp v9, v9 row_ror:8 row_mask:0xf bank_mask:0xf\n"
  "s_waitcnt lgkmcnt(3)\n"
  "v_mfma_f32_16x16x32_bf16 v[10:13], v[34:37], v[14:17], v[10:13]\n"
  "ds_read_b128 v[34:37], v49 offset:0\n"
  "v_mov_b32_dpp v22, v22 row_ror:8 row_mask:0xf bank_mask:0xf\n"
  "s_waitcnt lgkmcnt(3)\n"
  "v_mfma_f32_16x16x32_bf16 v[18:21], v[38:41], v[14:17], v[18:21]\n"
  "ds_read_b128 v[38:41], v49 offset:6144\n"
  "v_mov_b32_dpp v23, v23 row_ror:8 row_mask:0xf bank_mask:0xf\n"
  "s_waitcnt lgkmcnt(3)\n"
  "v_mfma_f32_16x16x32_bf16 v[26:29], v[42:45], v[14:17], v[26:29]\n"
  "ds_read_b128 v[42:45], v49 offset:12288\n"
  "v_mov_b32_dpp v24, v24 row_ror:8 row_mask:0xf bank_mask:0xf\n"
  "s_waitcnt lgkmcnt(3)\n"
  "v_mfma_f32_16x16x32_bf16 v[30:33], v[2:5], v[14:17], v[30:33]\n"
  "ds_read_b128 v[2:5], v49 offset:18432\n"
  "v_mov_b32_dpp v25, v25 row_ror:8 row_mask:0xf bank_mask:0xf\n"
  "s_waitcnt lgkmcnt(3)\n"
  "v_mfma_f32_16x16x32_bf16 v[10:13], v[34:37], v[6:9], v[10:13]\n"
  "ds_read_b128 v[34:37], v50 offset:0\n"
  "v_mov_b32_dpp v14, v14 row_ror:8 row_mask:0xf bank_mask:0xf\n"
  "s_waitcnt lgkmcnt(3)\n"
  "v_mfma_f32_16x16x32_bf16 v[18:21], v[38:41], v[6:9], v[18:21]\n"
  "ds_read_b128 v[38:41], v50 offset:6144\n"
  "v_mov_b32_dpp v15, v15 row_ror:8 row_mask:0xf bank_mask:0xf\n"
  "s_waitcnt lgkmcnt(3)\n"
  "v_mfma_f32_16x16x32_bf16 v[26:29], v[42:45], v[6:9], v[26:29]\n"
  "ds_read_b128 v[42:45], v50 offset:12288\n"
  "v_mov_b32_dpp v16, v16 row_ror:8 row_mask:0xf bank_mask:0xf\n"
  "s_waitcnt lgkmcnt(3)\n"
  "v_mfma_f32_16x16x32_bf16 v[30:33], v[2:5], v[6:9], v[30:33]\n"
  "ds_read_b128 v[2:5], v50 offset:18432\n"
  "v_mov_b32_dpp v17, v17 row_ror:8 row_mask:0xf bank_mask:0xf\n"
  "s_waitcnt lgkmcnt(3)\n"
  "v_mfma_f32_16x16x32_bf16 v[10:13], v[34:37], v[22:25], v[10:13]\n"
  "ds_read_b128 v[34:37], v51 offset:0\n"
  "s_waitcnt lgkmcnt(3)\n"
  "v_mfma_f32_16x16x32_bf16 v[18:21], v[38:41], v[22:25], v[18:21]\n"
  "ds_read_b128 v[38:41], v51 offset:6144\n"
  "s_waitcnt lgkmcnt(3)\n"
  "v_mfma_f32_16x16x32_bf16 v[26:29], v[42:45], v[22:25], v[26:29]\n"
  "ds_read_b128 v[42:45], v51 offset:12288\n"
  "s_waitcnt lgkmcnt(3)\n"
  "v_mfma_f32_16x16x32_bf16 v[30:33], v[2:5], v[22:25], v[30:33]\n"
  "ds_read_b128 v[2:5], v51 offset:18432\n"
  "s_waitcnt lgkmcnt(3)\n"
  "v_mfma_f32_16x16x32_bf16 v[10:13], v[34:37], v[14:17], v[10:13]\n"
  "s_waitcnt lgkmcnt(2)\n"
  "v_mfma_f32_16x16x32_bf16 v[18:21], v[38:41], v[14:17], v[18:21]\n"
  "s_waitcnt lgkmcnt(1)\n"
  "v_mfma_f32_16x16x32_bf16 v[26:29], v[42:45], v[14:17], v[26:29]\n"
  "s_waitcnt lgkmcnt(0)\n"
  "v_mfma_f32_16x16x32_bf16 v[30:33], v[2:5], v[14:17], v[30:33]\n"
  "v_lshrrev_b32 v50, 2, v0\n"
  "v_mul_u32_u24 v46, 384, v50\n"
  "v_and_b32 v50, 6, v50\n"
  "v_lshl_add_u32 v46, v50, 4, v46\n"
  "v_and_b32 v50, 3, v0\n"
  "v_lshl_add_u32 v46, v50, 3, v46\n"
  "v_add_u32 v46, s75, v46\n"
  "v_xor_b32 v47, 32, v46\n"
  "v_xor_b32 v48, 64, v46\n"
  "v_xor_b32 v49, 96, v46\n"
  "s_mov_b64 exec, s[78:79]\n"
  "ds_write_b128 v52, v[10:13] offset:0\n"
  "ds_write_b128 v52, v[18:21] offset:64\n"
  "ds_write_b128 v52, v[26:29] offset:128\n"
  "ds_write_b128 v52, v[30:33] offset:192\n"
  "s_mov_b64 exec, -1\n"
  "s_waitcnt lgkmcnt(0)\n"
  "s_barrier\n"
  "ds_read_b128 v[6:9], v53 offset:0\n"
  "ds_read_b128 v[10:13], v53 offset:64\n"
  "ds_read_b128 v[14:17], v53 offset:7680\n"
  "ds_read_b128 v[18:21], v53 offset:7744\n"
  "ds_read_b128 v[22:25], v53 offset:15360\n"
  "ds_read_b128 v[26:29], v53 offset:15424\n"
  "ds_read_b128 v[30:33], v53 offset:23040\n"
  "ds_read_b128 v[34:37], v53 offset:23104\n"
  "ds_read_b64_tr_b16 v[38:39], v46 offset:0\n"
  "ds_read_b64_tr_b16 v[40:41], v46 offset:6144\n"
  "ds_read_b64_tr_b16 v[42:43], v47 offset:0\n"
  "ds_read_b64_tr_b16 v[44:45], v47 offset:6144\n"
  "ds_read_b64_tr_b16 v[2:3], v48 offset:0\n"
  "ds_read_b64_tr_b16 v[4:5], v48 offset:6144\n"
  "s_waitcnt lgkmcnt(10)\n"
  "v_add_f32 v6, v6, v14\n"
  "v_add_f32 v7, v7, v15\n"
  "v_add_f32 v8, v8, v16\n"
  "v_add_f32 v9, v9, v17\n"
  "v_add_f32 v10, v10, v18\n"
  "v_add_f32 v11, v11, v19\n"
  "v_add_f32 v12, v12, v20\n"
  "v_add_f32 v13, v13, v21\n"
  "s_waitcnt lgkmcnt(8)\n"
  "v_add_f32 v6, v6, v22\n"
  "v_add_f32 v7, v7, v23\n"
  "v_add_f32 v8, v8, v24\n"
  "v_add_f32 v9, v9, v25\n"
  "v_add_f32 v10, v10, v26\n"
  "v_add_f32 v11, v11, v27\n"
  "v_add_f32 v12, v12, v28\n"
  "v_add_f32 v13, v13, v29\n"
  "s_waitcnt lgkmcnt(6)\n"
  "v_add_f32 v6, v6, v30\n"
  "v_add_f32 v7, v7, v31\n"
  "v_add_f32 v8, v8, v32\n"
  "v_add_f32 v9, v9, v33\n"
  "v_add_f32 v10, v10, v34\n"
  "v_add_f32 v11, v11, v35\n"
  "v_add_f32 v12, v12, v36\n"
  "v_add_f32 v13, v13, v37\n"
  "v_max3_f32 v50, v6, v7, v8\n"
  "v_max3_f32 v51, v9, v10, v11\n"
  "v_max3_f32 v50, v50, v12, v13\n"
  "v_max_f32 v50, v50, v51\n"
  "s_nop 1\n"
  "v_max_f32_dpp v50, v50, v50 row_ror:8 row_mask:0xf bank_mask:0xf\n"
  "v_mov_b32 v51, v50\n"
  "s_nop 1\n"
  "v_permlane16_swap_b32 v50, v51\n"
  "v_max_f32 v50, v50, v51\n"
  "v_mov_b32 v51, v50\n"
  "s_nop 1\n"
  "v_permlane32_swap_b32 v50, v51\n"
  "v_max_f32 v50, v50, v51\n"
  "v_mul_f32 v50, s80, v50\n"
  "v_fma_f32 v14, v6, s80, -v50\n"
  "v_fma_f32 v15, v7, s80, -v50\n"
  "v_fma_f32 v16, v8, s80, -v50\n"
  "v_fma_f32 v17, v9, s80, -v50\n"
  "v_fma_f32 v18, v10, s80, -v50\n"
  "v_fma_f32 v19, v11, s80, -v50\n"
  "v_fma_f32 v20, v12, s80, -v50\n"
  "v_fma_f32 v21, v13, s80, -v50\n"
  "v_exp_f32 v14, v14\n"
  "v_exp_f32 v15, v15\n"
  "v_exp_f32 v16, v16\n"
  "v_exp_f32 v17, v17\n"
  "v_exp_f32 v18, v18\n"
  "v_exp_f32 v19, v19\n"
  "v_exp_f32 v20, v20\n"
  "v_exp_f32 v21, v21\n"
  "v_cvt_pk_bf16_f32 v10, v14, v15\n"
  "v_cvt_pk_bf16_f32 v11, v16, v17\n"
  "v_cvt_pk_bf16_f32 v12, v18, v19\n"
  "v_cvt_pk_bf16_f32 v13, v20, v21\n"
  "v_cndmask_b32_e64 v6, 0, v10, s[78:79]\n"
  "v_cndmask_b32_e64 v7, 0, v11, s[78:79]\n"
  "v_cndmask_b32_e64 v8, 0, v12, s[78:79]\n"
  "v_cndmask_b32_e64 v9, 0, v13, s[78:79]\n"
  "v_add_f32 v51, v14, v15\n"
  "v_add_f32 v52, v16, v17\n"
  "v_add_f32 v53, v18, v19\n"
  "v_add_f32 v54, v20, v21\n"
  "s_waitcnt lgkmcnt(4)\n"
  "v_mfma_f32_16x16x32_bf16 v[14:17], v[6:9], v[38:41], 0\n"
  "ds_read_b64_tr_b16 v[38:39], v49 offset:0\n"
  "ds_read_b64_tr_b16 v[40:41], v49 offset:6144\n"
  "v_add_f32 v51, v51, v52\n"
  "v_add_f32 v53, v53, v54\n"
  "s_waitcnt lgkmcnt(4)\n"
  "v_mfma_f32_16x16x32_bf16 v[18:21], v[6:9], v[42:45], 0\n"
  "ds_read_b64_tr_b16 v[42:43], v46 offset:128\n"
  "ds_read_b64_tr_b16 v[44:45], v46 offset:6272\n"
  "v_cndmask_b32_e64 v10, v10, 0, s[78:79]\n"
  "v_cndmask_b32_e64 v11, v11, 0, s[78:79]\n"
  "s_waitcnt lgkmcnt(4)\n"
  "v_mfma_f32_16x16x32_bf16 v[22:25], v[6:9], v[2:5], 0\n"
  "ds_read_b64_tr_b16 v[2:3], v47 offset:128\n"
  "ds_read_b64_tr_b16 v[4:5], v47 offset:6272\n"
  "v_cndmask_b32_e64 v12, v12, 0, s[78:79]\n"
  "v_cndmask_b32_e64 v13, v13, 0, s[78:79]\n"
  "s_waitcnt lgkmcnt(4)\n"
  "v_mfma_f32_16x16x32_bf16 v[26:29], v[6:9], v[38:41], 0\n"
  "ds_read_b64_tr_b16 v[38:39], v48 offset:12416\n"
  "ds_read_b64_tr_b16 v[40:41], v48 offset:18560\n"
  "v_add_f32 v51, v51, v53\n"
  "s_waitcnt lgkmcnt(4)\n"
  "v_mfma_f32_16x16x32_bf16 v[30:33], v[6:9], v[42:45], 0\n"
  "ds_read_b64_tr_b16 v[42:43], v49 offset:12416\n"
  "ds_read_b64_tr_b16 v[44:45], v49 offset:18560\n"
  "v_add_f32_dpp v51, v51, v51 row_ror:8 row_mask:0xf bank_mask:0xf\n"
  "s_waitcnt lgkmcnt(4)\n"
  "v_mfma_f32_16x16x32_bf16 v[34:37], v[6:9], v[2:5], 0\n"
  "ds_read_b64_tr_b16 v[2:3], v46 offset:12544\n"
  "ds_read_b64_tr_b16 v[4:5], v46 offset:18688\n"
  "s_waitcnt lgkmcnt(4)\n"
  "v_mfma_f32_16x16x32_bf16 v[14:17], v[10:13], v[38:41], v[14:17]\n"
  "ds_read_b64_tr_b16 v[38:39], v47 offset:12544\n"
  "ds_read_b64_tr_b16 v[40:41], v47 offset:18688\n"
  "v_mov_b32_dpp v6, v6 row_ror:8 row_mask:0xf bank_mask:0xf\n"
  "v_mov_b32_dpp v7, v7 row_ror:8 row_mask:0xf bank_mask:0xf\n"
  "s_waitcnt lgkmcnt(4)\n"
  "v_mfma_f32_16x16x32_bf16 v[18:21], v[10:13], v[42:45], v[18:21]\n"
  "ds_read_b64_tr_b16 v[42:43], v48 offset:12544\n"
  "ds_read_b64_tr_b16 v[44:45], v48 offset:18688\n"
  "v_mov_b32_dpp v8, v8 row_ror:8 row_mask:0xf bank_mask:0xf\n"
  "v_mov_b32_dpp v9, v9 row_ror:8 row_mask:0xf bank_mask:0xf\n"
  "s_waitcnt lgkmcnt(4)\n"
  "v_mfma_f32_16x16x32_bf16 v[22:25], v[10:13], v[2:5], v[22:25]\n"
  "ds_read_b64_tr_b16 v[2:3], v49 offset:12544\n"
  "ds_read_b64_tr_b16 v[4:5], v49 offset:18688\n"
  "v_mov_b32 v52, v51\n"
  "s_nop 1\n"
  "v_permlane16_swap_b32 v51, v52\n"
  "v_add_f32 v51, v51, v52\n"
  "s_waitcnt lgkmcnt(4)\n"
  "v_mfma_f32_16x16x32_bf16 v[26:29], v[10:13], v[38:41], v[26:29]\n"
  "ds_read_b64_tr_b16 v[38:39], v48 offset:128\n"
  "ds_read_b64_tr_b16 v[40:41], v48 offset:6272\n"
  "v_mov_b32 v52, v51\n"
  "s_nop 1\n"
  "v_permlane32_swap_b32 v51, v52\n"
  "v_add_f32 v51, v51, v52\n"
  "s_waitcnt lgkmcnt(4)\n"
  "v_mfma_f32_16x16x32_bf16 v[30:33], v[10:13], v[42:45], v[30:33]\n"
  "ds_read_b64_tr_b16 v[42:43], v49 offset:128\n"
  "ds_read_b64_tr_b16 v[44:45], v49 offset:6272\n"
  "v_mov_b32 v53, s62\n"
  "v_lshlrev_b32 v54, 10, v0\n"
  "s_cmp_lg_u32 s33, 3\n"
  "s_cbranch_scc1 Lsk_17\n"
  "s_mov_b32 exec_lo, 0xff\n"
  "s_mov_b32 exec_hi, 0x0\n"
  "buffer_store_dwordx4 v[50:53], v54, s[56:59], s94 offen sc1\n"
  "s_mov_b64 exec, -1\n"
  "Lsk_17:\n"
  "s_waitcnt lgkmcnt(4)\n"
  "v_mfma_f32_16x16x32_bf16 v[34:37], v[10:13], v[2:5], v[34:37]\n"
  "ds_read_b64_tr_b16 v[2:3], v46 offset:256\n"
  "ds_read_b64_tr_b16 v[4:5], v46 offset:6400\n"
  "s_waitcnt lgkmcnt(4)\n"
  "v_mfma_f32_16x16x32_bf16 v[14:17], v[6:9], v[38:41], v[14:17]\n"
  "ds_read_b64_tr_b16 v[38:39], v47 offset:256\n"
  "ds_read_b64_tr_b16 v[40:41], v47 offset:6400\n"
  "v_mov_b32_dpp v10, v10 row_ror:8 row_mask:0xf bank_mask:0xf\n"
  "v_mov_b32_dpp v11, v11 row_ror:8 row_mask:0xf bank_mask:0xf\n"
  "s_waitcnt lgkmcnt(4)\n"
  "v_mfma_f32_16x16x32_bf16 v[18:21], v[6:9], v[42:45], v[18:21]\n"
  "ds_read_b64_tr_b16 v[42:43], v48 offset:256\n"
  "ds_read_b64_tr_b16 v[44:45], v48 offset:6400\n"
  "v_mov_b32_dpp v12, v12 row_ror:8 row_mask:0xf bank_mask:0xf\n"
  "v_mov_b32_dpp v13, v13 row_ror:8 row_mask:0xf bank_mask:0xf\n"
  "s_waitcnt lgkmcnt(4)\n"
  "v_mfma_f32_16x16x32_bf16 v[22:25], v[6:9], v[2:5], v[22:25]\n"
  "ds_read_b64_tr_b16 v[2:3], v49 offset:256\n"
  "ds_read_b64_tr_b16 v[4:5], v49 offset:6400\n"
  "v_and_b32 v1, 16, v0\n"
  "v_mul_u32_u24 v1, 32768, v1\n"
  "s_waitcnt lgkmcnt(4)\n"
  "v_mfma_f32_16x16x32_bf16 v[26:29], v[6:9], v[38:41], v[26:29]\n"
  "ds_read_b64_tr_b16 v[38:39], v46 offset:12288\n"
  "ds_read_b64_tr_b16 v[40:41], v46 offset:18432\n"
  "v_and_b32 v54, 32, v0\n"
  "v_mad_u32_u24 v1, v54, 8, v1\n"
  "s_waitcnt lgkmcnt(4)\n"
  "v_mfma_f32_16x16x32_bf16 v[30:33], v[6:9], v[42:45], v[30:33]\n"
  "ds_read_b64_tr_b16 v[42:43], v47 offset:12288\n"
  "ds_read_b64_tr_b16 v[44:45], v47 offset:18432\n"
  "v_and_b32 v54, 15, v0\n"
  "v_mad_u32_u24 v1, v54, 16, v1\n"
  "s_waitcnt lgkmcnt(4)\n"
  "v_mfma_f32_16x16x32_bf16 v[34:37], v[6:9], v[2:5], v[34:37]\n"
  "ds_read_b64_tr_b16 v[2:3], v48 offset:12288\n"
  "ds_read_b64_tr_b16 v[4:5], v48 offset:18432\n"
  "s_waitcnt lgkmcnt(4)\n"
  "v_mfma_f32_16x16x32_bf16 v[14:17], v[10:13], v[38:41], v[14:17]\n"
  "ds_read_b64_tr_b16 v[38:39], v49 offset:12288\n"
  "ds_read_b64_tr_b16 v[40:41], v49 offset:18432\n"
  "s_waitcnt lgkmcnt(4)\n"
  "v_mfma_f32_16x16x32_bf16 v[18:21], v[10:13], v[42:45], v[18:21]\n"
  "ds_read_b64_tr_b16 v[42:43], v46 offset:12416\n"
  "ds_read_b64_tr_b16 v[44:45], v46 offset:18560\n"
  "s_waitcnt lgkmcnt(4)\n"
  "v_mfma_f32_16x16x32_bf16 v[22:25], v[10:13], v[2:5], v[22:25]\n"
  "ds_read_b64_tr_b16 v[2:3], v47 offset:12416\n"
  "ds_read_b64_tr_b16 v[4:5], v47 offset:18560\n"
  "s_waitcnt lgkmcnt(4)\n"
  "v_mfma_f32_16x16x32_bf16 v[26:29], v[10:13], v[38:41], v[26:29]\n"
  "s_waitcnt lgkmcnt(2)\n"
  "v_mfma_f32_16x16x32_bf16 v[30:33], v[10:13], v[42:45], v[30:33]\n"
  "s_waitcnt lgkmcnt(0)\n"
  "v_mfma_f32_16x16x32_bf16 v[34:37], v[10:13], v[2:5], v[34:37]\n"
  "v_mov_b32 v49, s62\n"
  "v_mov_b32 v41, s62\n"
  "s_add_u32 s89, s93, 131072\n"
  "v_mov_b32 v45, s62\n"
  "s_add_u32 s90, s93, 262144\n"
  "v_mov_b32 v9, s62\n"
  "s_add_u32 s91, s93, 393216\n"
  "v_cvt_pk_bf16_f32 v46, v14, v18\n"
  "v_cvt_pk_bf16_f32 v47, v22, v26\n"
  "v_cvt_pk_bf16_f32 v48, v30, v34\n"
  "buffer_store_dwordx4 v[46:49], v1, s[56:59], s93 offen sc1\n"
  "v_cvt_pk_bf16_f32 v38, v15, v19\n"
  "v_cvt_pk_bf16_f32 v39, v23, v27\n"
  "v_cvt_pk_bf16_f32 v40, v31, v35\n"
  "buffer_store_dwordx4 v[38:41], v1, s[56:59], s89 offen sc1\n"
  "v_cvt_pk_bf16_f32 v42, v16, v20\n"
  "v_cvt_pk_bf16_f32 v43, v24, v28\n"
  "v_cvt_pk_bf16_f32 v44, v32, v36\n"
  "buffer_store_dwordx4 v[42:45], v1, s[56:59], s90 offen sc1\n"
  "v_cvt_pk_bf16_f32 v6, v17, v21\n"
  "v_cvt_pk_bf16_f32 v7, v25, v29\n"
  "v_cvt_pk_bf16_f32 v8, v33, v37\n"
  "buffer_store_dwordx4 v[6:9], v1, s[56:59], s91 offen sc1\n"
  "s_waitcnt lgkmcnt(0)\n"
  "s_barrier\n"
  "s_add_u32 s70, s84, 0\n"
  "v_add_u32 v24, s70, v0\n"
  "v_mul_u32_u24 v25, 6554, v24\n"
  "v_lshrrev_b32 v25, 16, v25\n"
  "v_mul_u32_u24 v26, 10, v25\n"
  "v_sub_u32 v24, v24, v26\n"
  "v_lshrrev_b32 v26, 1, v25\n"
  "v_xor_b32 v26, v26, v25\n"
  "v_and_b32 v26, 4, v26\n"
  "v_mul_u32_u24 v26, 3, v26\n"
  "v_xor_b32 v25, v25, v26\n"
  "v_mul_u32_u24 v26, 1280, v25\n"
  "v_lshl_add_u32 v26, v24, 4, v26\n"
  "s_add_u32 s70, s84, 64\n"
  "v_add_u32 v24, s70, v0\n"
  "v_mul_u32_u24 v25, 6554, v24\n"
  "v_lshrrev_b32 v25, 16, v25\n"
  "v_mul_u32_u24 v27, 10, v25\n"
  "v_sub_u32 v24, v24, v27\n"
  "v_lshrrev_b32 v27, 1, v25\n"
  "v_xor_b32 v27, v27, v25\n"
  "v_and_b32 v27, 4, v27\n"
  "v_mul_u32_u24 v27, 3, v27\n"
  "v_xor_b32 v25, v25, v27\n"
  "v_mul_u32_u24 v27, 1280, v25\n"
  "v_lshl_add_u32 v27, v24, 4, v27\n"
  "s_add_u32 s70, s84, 128\n"
  "v_add_u32 v24, s70, v0\n"
  "v_mul_u32_u24 v25, 6554, v24\n"
  "v_lshrrev_b32 v25, 16, v25\n"
  "v_mul_u32_u24 v28, 10, v25\n"
  "v_sub_u32 v24, v24, v28\n"
  "v_lshrrev_b32 v28, 1, v25\n"
  "v_xor_b32 v28, v28, v25\n"
  "v_and_b32 v28, 4, v28\n"
  "v_mul_u32_u24 v28, 3, v28\n"
  "v_xor_b32 v25, v25, v28\n"
  "v_mul_u32_u24 v28, 1280, v25\n"
  "v_lshl_add_u32 v28, v24, 4, v28\n"
  "s_add_u32 s70, s84, 192\n"
  "v_add_u32 v24, s70, v0\n"
  "v_mul_u32_u24 v25, 6554, v24\n"
  "v_lshrrev_b32 v25, 16, v25\n"
  "v_mul_u32_u24 v29, 10, v25\n"
  "v_sub_u32 v24, v24, v29\n"
  "v_lshrrev_b32 v29, 1, v25\n"
  "v_xor_b32 v29, v29, v25\n"
  "v_and_b32 v29, 4, v29\n"
  "v_mul_u32_u24 v29, 3, v29\n"
  "v_xor_b32 v25, v25, v29\n"
  "v_mul_u32_u24 v29, 1280, v25\n"
  "v_lshl_add_u32 v29, v24, 4, v29\n"
  "s_add_u32 s70, s84, 256\n"
  "v_add_u32 v24, s70, v0\n"
  "v_mul_u32_u24 v25, 6554, v24\n"
  "v_lshrrev_b32 v25, 16, v25\n"
  "v_mul_u32_u24 v30, 10, v25\n"
  "v_sub_u32 v24, v24, v30\n"
  "v_lshrrev_b32 v30, 1, v25\n"
  "v_xor_b32 v30, v30, v25\n"
  "v_and_b32 v30, 4, v30\n"
  "v_mul_u32_u24 v30, 3, v30\n"
  "v_xor_b32 v25, v25, v30\n"
  "v_mul_u32_u24 v30, 1280, v25\n"
  "v_lshl_add_u32 v30, v24, 4, v30\n"
  "s_lshl_b32 s70, s84, 4\n"
  "s_add_u32 m0, s70, 130048\n"
  "s_add_u32 s71, s82, 0\n"
  "buffer_load_dwordx4 v27, s[48:51], s71 offen nt lds\n"
  "s_lshl_b32 s70, s84, 4\n"
  "s_add_u32 m0, s70, 131072\n"
  "s_add_u32 s71, s82, 0\n"
  "buffer_load_dwordx4 v28, s[48:51], s71 offen nt lds\n"
  "s_lshl_b32 s70, s84, 4\n"
  "s_add_u32 m0, s70, 132096\n"
  "s_add_u32 s71, s82, 0\n"
  "buffer_load_dwordx4 v29, s[48:51], s71 offen nt lds\n"
  "s_mul_i32 s70, s33, 9216\n"
  "s_add_u32 s70, s70, 0\n"
  "v_lshlrev_b32 v1, 4, v0\n"
  "v_add_u32 v1, s70, v1\n"
  "v_and_b32 v4, 4, v0\n"
  "v_lshlrev_b32 v4, 2, v4\n"
  "v_lshrrev_b32 v5, 4, v0\n"
  "v_lshl_add_u32 v4, v5, 2, v4\n"
  "v_lshrrev_b32 v5, 3, v0\n"
  "v_and_b32 v5, 1, v5\n"
  "v_add_u32 v4, v4, v5\n"
  "v_add_u32 v5, 2, v4\n"
  "s_lshl_b32 s70, s33, 1\n"
  "v_and_b32 v3, 3, v0\n"
  "v_and_b32 v2, 6, v4\n"
  "v_xor_b32 v2, s70, v2\n"
  "v_mul_u32_u24 v4, 384, v4\n"
  "v_lshl_add_u32 v4, v2, 4, v4\n"
  "v_lshl_add_u32 v4, v3, 3, v4\n"
  "v_and_b32 v2, 6, v5\n"
  "v_xor_b32 v2, s70, v2\n"
  "v_mul_u32_u24 v5, 384, v5\n"
  "v_lshl_add_u32 v5, v2, 4, v5\n"
  "v_lshl_add_u32 v5, v3, 3, v5\n"
  "s_waitcnt vmcnt(7)\n"
  "ds_write_b128 v1, v[220:223] offset:0\n"
  "ds_write_b128 v1, v[224:227] offset:1024\n"
  "ds_write_b128 v1, v[228:231] offset:2048\n"
  "ds_write_b128 v1, v[232:235] offset:3072\n"
  "ds_write_b128 v1, v[236:239] offset:4096\n"
  "ds_write_b128 v1, v[240:243] offset:5120\n"
  "ds_write_b128 v1, v[244:247] offset:6144\n"
  "ds_write_b128 v1, v[248:251] offset:7168\n"
  "ds_write_b128 v1, v[252:255] offset:8192\n"
  "v_and_b32 v45, 12, v0\n"
  "v_lshrrev_b32 v46, 4, v0\n"
  "v_add_u32 v45, v45, v46\n"
  "v_lshlrev_b32 v45, 4, v45\n"
  "v_and_b32 v47, 15, v0\n"
  "v_mul_u32_u24 v46, 512, v46\n"
  "v_mad_u32_u24 v46, v47, 16, v46\n"
  "s_lshl_b32 s70, s34, 2\n"
  "s_add_u32 s70, s70, s33\n"
  "s_lshl_b32 s70, s70, 8\n"
  "s_add_u32 s88, s70, 851968\n"
  "s_lshr_b32 s70, s83, 1\n"
  "s_lshl_b32 s70, s70, 2\n"
  "s_add_u32 s70, s70, s33\n"
  "s_mul_i32 s70, s70, 8192\n"
  "s_and_b32 s71, s35, 1\n"
  "s_mul_i32 s71, s71, 256\n"
  "s_add_u32 s70, s70, s71\n"
  "s_add_u32 s89, s70, 4194304\n"
  "s_add_u32 s90, s89, 4096\n"
  "v_and_b32 v47, 1, v0\n"
  "v_lshlrev_b32 v47, 4, v47\n"
  "v_mov_b32 v48, 0xffff\n"
  "v_lshlrev_b32 v47, v47, v48\n"
  "s_mov_b32 s66, 0x30003\n"
  "s_mov_b32 s67, 0x30003\n"
  "v_cndmask_b32_e64 v42, 0, v47, s[66:67]\n"
  "s_mov_b32 s66, 0x300030\n"
  "s_mov_b32 s67, 0x300030\n"
  "v_cndmask_b32_e64 v43, 0, v47, s[66:67]\n"
  "s_mov_b32 s66, 0x3000300\n"
  "s_mov_b32 s67, 0x3000300\n"
  "v_cndmask_b32_e64 v44, 0, v47, s[66:67]\n"
  "v_mov_b32 v29, 0\n"
  "v_mov_b32 v33, 0\n"
  "v_mov_b32 v37, 0\n"
  "v_mov_b32 v41, 0\n"
  "v_lshlrev_b32 v52, 3, v0\n"
  "v_add_u32 v48, 36864, v52\n"
  "s_lshl_b32 s70, s33, 9\n"
  "v_add_u32 v47, s70, v48\n"
  "s_lshl_b32 s70, s33, 3\n"
  "s_add_u32 s70, s70, 162432\n"
  "v_mov_b32 v49, s70\n"
  "s_lshl_b32 s70, s33, 8\n"
  "s_add_u32 s70, s70, 38912\n"
  "v_lshlrev_b32 v50, 2, v0\n"
  "v_add_u32 v50, s70, v50\n"
  "v_and_b32 v51, 0x30, v0\n"
  "v_add_u32 v51, s70, v51\n"
  "s_lshl_b32 s70, s83, 10\n"
  "s_lshl_b32 s71, s33, 7\n"
  "s_add_u32 s70, s70, s71\n"
  "s_add_u32 s76, s70, 1179648\n"
  "LCB3:\n"
  "s_lshl_b32 s99, s33, 3\n"
  "s_lshr_b32 s98, 0x1a1a1a1a, s99\n"
  "s_and_b32 s98, s98, 0xff\n"
  "Ldl_18:\n"
  "s_sub_u32 s98, s98, 1\n"
  "s_cbranch_scc0 Ldl_18\n"
  "buffer_load_dwordx4 v[6:9], v45, s[56:59], s88 offen sc1\n"
  "buffer_load_dwordx4 v[10:13], v46, s[56:59], s89 offen offset:0 sc1\n"
  "buffer_load_dwordx4 v[14:17], v46, s[56:59], s89 offen offset:2048 sc1\n"
  "buffer_load_dwordx4 v[18:21], v46, s[56:59], s90 offen offset:0 sc1\n"
  "buffer_load_dwordx4 v[22:25], v46, s[56:59], s90 offen offset:2048 sc1\n"
  "s_waitcnt lgkmcnt(0)\n"
  "s_barrier\n"
  "ds_read_b64_tr_b16 v[220:221], v4 offset:0\n"
  "ds_read_b64_tr_b16 v[222:223], v5 offset:0\n"
  "ds_read_b64_tr_b16 v[224:225], v4 offset:12288\n"
  "ds_read_b64_tr_b16 v[226:227], v5 offset:12288\n"
  "ds_read_b64_tr_b16 v[228:229], v4 offset:24576\n"
  "ds_read_b64_tr_b16 v[230:231], v5 offset:24576\n"
  "ds_read_b64_tr_b16 v[232:233], v4 offset:128\n"
  "ds_read_b64_tr_b16 v[234:235], v5 offset:128\n"
  "ds_read_b64_tr_b16 v[236:237], v4 offset:12416\n"
  "ds_read_b64_tr_b16 v[238:239], v5 offset:12416\n"
  "ds_read_b64_tr_b16 v[240:241], v4 offset:24704\n"
  "ds_read_b64_tr_b16 v[242:243], v5 offset:24704\n"
  "s_waitcnt lgkmcnt(0)\n"
  "ds_read_b64_tr_b16 v[244:245], v4 offset:256\n"
  "ds_read_b64_tr_b16 v[246:247], v5 offset:256\n"
  "ds_read_b64_tr_b16 v[248:249], v4 offset:12544\n"
  "ds_read_b64_tr_b16 v[250:251], v5 offset:12544\n"
  "ds_read_b64_tr_b16 v[252:253], v4 offset:24832\n"
  "ds_read_b64_tr_b16 v[254:255], v5 offset:24832\n"
  "LPB3_19:\n"
  "s_waitcnt vmcnt(4)\n"
  "v_cmp_ne_u32_e64 s[64:65], s62, v9\n"
  "v_mov_b32 v2, v6\n"
  "s_nop 1\n"
  "v_max_f32_dpp v2, v2, v2 row_ror:4 row_mask:0xf bank_mask:0xf\n"
  "s_nop 1\n"
  "v_max_f32_dpp v2, v2, v2 row_ror:8 row_mask:0xf bank_mask:0xf\n"
  "v_mov_b32 v8, v2\n"
  "s_nop 1\n"
  "v_permlane16_swap_b32 v2, v8\n"
  "v_max_f32 v2, v2, v8\n"
  "v_mov_b32 v8, v2\n"
  "s_nop 1\n"
  "v_permlane32_swap_b32 v2, v8\n"
  "v_max_f32 v2, v2, v8\n"
  "v_sub_f32 v6, v6, v2\n"
  "v_exp_f32 v6, v6\n"
  "s_nop 0\n"
  "v_cvt_pk_bf16_f32 v6, v6, v6\n"
  "s_nop 1\n"
  "v_and_b32_dpp v26, v6, v42 row_newbcast:0 row_mask:0xf bank_mask:0xf\n"
  "v_and_b32_dpp v27, v6, v43 row_newbcast:0 row_mask:0xf bank_mask:0xf\n"
  "v_and_b32_dpp v28, v6, v44 row_newbcast:0 row_mask:0xf bank_mask:0xf\n"
  "v_and_b32_dpp v30, v6, v42 row_newbcast:4 row_mask:0xf bank_mask:0xf\n"
  "v_and_b32_dpp v31, v6, v43 row_newbcast:4 row_mask:0xf bank_mask:0xf\n"
  "v_and_b32_dpp v32, v6, v44 row_newbcast:4 row_mask:0xf bank_mask:0xf\n"
  "v_and_b32_dpp v34, v6, v42 row_newbcast:8 row_mask:0xf bank_mask:0xf\n"
  "v_and_b32_dpp v35, v6, v43 row_newbcast:8 row_mask:0xf bank_mask:0xf\n"
  "v_and_b32_dpp v36, v6, v44 row_newbcast:8 row_mask:0xf bank_mask:0xf\n"
  "v_and_b32_dpp v38, v6, v42 row_newbcast:12 row_mask:0xf bank_mask:0xf\n"
  "v_and_b32_dpp v39, v6, v43 row_newbcast:12 row_mask:0xf bank_mask:0xf\n"
  "v_and_b32_dpp v40, v6, v44 row_newbcast:12 row_mask:0xf bank_mask:0xf\n"
  "v_and_b32 v8, 0xffff0000, v6\n"
  "v_mul_f32 v3, v8, v7\n"
  "s_nop 1\n"
  "v_add_f32_dpp v3, v3, v3 row_ror:4 row_mask:0xf bank_mask:0xf\n"
  "s_nop 1\n"
  "v_add_f32_dpp v3, v3, v3 row_ror:8 row_mask:0xf bank_mask:0xf\n"
  "v_mov_b32 v8, v3\n"
  "s_nop 1\n"
  "v_permlane16_swap_b32 v3, v8\n"
  "v_add_f32 v3, v3, v8\n"
  "v_mov_b32 v8, v3\n"
  "s_nop 1\n"
  "v_permlane32_swap_b32 v3, v8\n"
  "v_add_f32 v3, v3, v8\n"
  "s_waitcnt vmcnt(3)\n"
  "v_mfma_f32_16x16x32_bf16 v[6:9], v[26:29], v[10:13], 0\n"
  "v_cmp_ne_u32 vcc, s62, v13\n"
  "s_nop 0\n"
  "s_or_b64 s[64:65], s[64:65], vcc\n"
  "s_waitcnt vmcnt(2)\n"
  "v_mfma_f32_16x16x32_bf16 v[6:9], v[30:33], v[14:17], v[6:9]\n"
  "v_cmp_ne_u32 vcc, s62, v17\n"
  "s_nop 0\n"
  "s_or_b64 s[64:65], s[64:65], vcc\n"
  "s_waitcnt vmcnt(1)\n"
  "v_mfma_f32_16x16x32_bf16 v[6:9], v[34:37], v[18:21], v[6:9]\n"
  "v_cmp_ne_u32 vcc, s62, v21\n"
  "s_nop 0\n"
  "s_or_b64 s[64:65], s[64:65], vcc\n"
  "s_waitcnt vmcnt(0)\n"
  "v_mfma_f32_16x16x32_bf16 v[6:9], v[38:41], v[22:25], v[6:9]\n"
  "v_cmp_ne_u32 vcc, s62, v25\n"
  "s_nop 0\n"
  "s_or_b64 s[64:65], s[64:65], vcc\n"
  "s_cbranch_scc1 LSB3_20\n"
  "ds_write_b64 v49, v[2:3]\n"
  "v_mov_b32 v53, 162432\n"
  "s_nop 1\n"
  "ds_write_b64 v47, v[6:7]\n"
  "s_waitcnt lgkmcnt(0)\n"
  "s_barrier\n"
  "ds_read_b128 v[18:21], v53\n"
  "ds_read_b128 v[22:25], v53 offset:16\n"
  "ds_read_b64 v[10:11], v48 offset:0\n"
  "ds_read_b64 v[12:13], v48 offset:512\n"
  "ds_read_b64 v[14:15], v48 offset:1024\n"
  "ds_read_b64 v[16:17], v48 offset:1536\n"
  "s_waitcnt lgkmcnt(4)\n"
  "v_max3_f32 v2, v18, v20, v22\n"
  "v_max_f32 v2, v2, v24\n"
  "v_sub_f32 v18, v18, v2\n"
  "v_sub_f32 v20, v20, v2\n"
  "v_sub_f32 v22, v22, v2\n"
  "v_sub_f32 v24, v24, v2\n"
  "v_exp_f32 v18, v18\n"
  "v_exp_f32 v20, v20\n"
  "v_exp_f32 v22, v22\n"
  "v_exp_f32 v24, v24\n"
  "s_waitcnt lgkmcnt(0)\n"
  "v_mul_f32 v10, v18, v10\n"
  "v_mul_f32 v11, v18, v11\n"
  "v_fmac_f32 v10, v20, v12\n"
  "v_fmac_f32 v11, v20, v13\n"
  "v_fmac_f32 v10, v22, v14\n"
  "v_fmac_f32 v11, v22, v15\n"
  "v_fmac_f32 v10, v24, v16\n"
  "v_fmac_f32 v11, v24, v17\n"
  "v_cvt_pk_bf16_f32 v10, v10, v11\n"
  "ds_write_b32 v50, v10\n"
  "ds_read_b128 v[26:29], v51 offset:0\n"
  "ds_read_b128 v[30:33], v51 offset:64\n"
  "ds_read_b128 v[34:37], v51 offset:128\n"
  "v_mul_f32 v19, v18, v19\n"
  "v_fmac_f32 v19, v20, v21\n"
  "v_fmac_f32 v19, v22, v23\n"
  "v_fmac_f32 v19, v24, v25\n"
  "v_rcp_f32 v19, v19\n"
  "v_mov_b32 v41, s62\n"
  "s_waitcnt lgkmcnt(2)\n"
  "v_mfma_f32_16x16x32_bf16 v[6:9], v[26:29], v[220:223], 0\n"
  "v_mfma_f32_16x16x32_bf16 v[10:13], v[26:29], v[232:235], 0\n"
  "v_mfma_f32_16x16x32_bf16 v[14:17], v[26:29], v[244:247], 0\n"
  "s_waitcnt lgkmcnt(1)\n"
  "v_mfma_f32_16x16x32_bf16 v[6:9], v[30:33], v[224:227], v[6:9]\n"
  "v_mfma_f32_16x16x32_bf16 v[10:13], v[30:33], v[236:239], v[10:13]\n"
  "v_mfma_f32_16x16x32_bf16 v[14:17], v[30:33], v[248:251], v[14:17]\n"
  "s_waitcnt lgkmcnt(0)\n"
  "v_mfma_f32_16x16x32_bf16 v[6:9], v[34:37], v[228:231], v[6:9]\n"
  "v_mfma_f32_16x16x32_bf16 v[10:13], v[34:37], v[240:243], v[10:13]\n"
  "v_mfma_f32_16x16x32_bf16 v[14:17], v[34:37], v[252:255], v[14:17]\n"
  "s_nop 5\n"
  "v_mul_f32 v6, v19, v6\n"
  "v_mul_f32 v10, v19, v10\n"
  "v_mul_f32 v14, v19, v14\n"
  "v_mov_b32_dpp v7, v6 quad_perm:[1,0,3,2] row_mask:0xf bank_mask:0xf\n"
  "v_mov_b32_dpp v11, v10 quad_perm:[1,0,3,2] row_mask:0xf bank_mask:0xf\n"
  "v_mov_b32_dpp v15, v14 quad_perm:[1,0,3,2] row_mask:0xf bank_mask:0xf\n"
  "v_cvt_pk_bf16_f32 v38, v6, v7\n"
  "v_cvt_pk_bf16_f32 v39, v10, v11\n"
  "v_cvt_pk_bf16_f32 v40, v14, v15\n"
  "s_mov_b32 exec_lo, 0x5555\n"
  "s_mov_b32 exec_hi, 0x0\n"
  "buffer_store_dwordx4 v[38:41], v52, s[56:59], s76 offen sc1\n"
  "s_mov_b64 exec, -1\n"
  "s_add_u32 s70, s84, 0\n"
  "v_add_u32 v24, s70, v0\n"
  "v_mul_u32_u24 v25, 6554, v24\n"
  "v_lshrrev_b32 v25, 16, v25\n"
  "v_mul_u32_u24 v26, 10, v25\n"
  "v_sub_u32 v24, v24, v26\n"
  "v_lshrrev_b32 v26, 1, v25\n"
  "v_xor_b32 v26, v26, v25\n"
  "v_and_b32 v26, 4, v26\n"
  "v_mul_u32_u24 v26, 3, v26\n"
  "v_xor_b32 v25, v25, v26\n"
  "v_mul_u32_u24 v26, 1280, v25\n"
  "v_lshl_add_u32 v26, v24, 4, v26\n"
  "s_add_u32 s70, s84, 64\n"
  "v_add_u32 v24, s70, v0\n"
  "v_mul_u32_u24 v25, 6554, v24\n"
  "v_lshrrev_b32 v25, 16, v25\n"
  "v_mul_u32_u24 v27, 10, v25\n"
  "v_sub_u32 v24, v24, v27\n"
  "v_lshrrev_b32 v27, 1, v25\n"
  "v_xor_b32 v27, v27, v25\n"
  "v_and_b32 v27, 4, v27\n"
  "v_mul_u32_u24 v27, 3, v27\n"
  "v_xor_b32 v25, v25, v27\n"
  "v_mul_u32_u24 v27, 1280, v25\n"
  "v_lshl_add_u32 v27, v24, 4, v27\n"
  "s_add_u32 s70, s84, 128\n"
  "v_add_u32 v24, s70, v0\n"
  "v_mul_u32_u24 v25, 6554, v24\n"
  "v_lshrrev_b32 v25, 16, v25\n"
  "v_mul_u32_u24 v28, 10, v25\n"
  "v_sub_u32 v24, v24, v28\n"
  "v_lshrrev_b32 v28, 1, v25\n"
  "v_xor_b32 v28, v28, v25\n"
  "v_and_b32 v28, 4, v28\n"
  "v_mul_u32_u24 v28, 3, v28\n"
  "v_xor_b32 v25, v25, v28\n"
  "v_mul_u32_u24 v28, 1280, v25\n"
  "v_lshl_add_u32 v28, v24, 4, v28\n"
  "s_add_u32 s70, s84, 192\n"
  "v_add_u32 v24, s70, v0\n"
  "v_mul_u32_u24 v25, 6554, v24\n"
  "v_lshrrev_b32 v25, 16, v25\n"
  "v_mul_u32_u24 v29, 10, v25\n"
  "v_sub_u32 v24, v24, v29\n"
  "v_lshrrev_b32 v29, 1, v25\n"
  "v_xor_b32 v29, v29, v25\n"
  "v_and_b32 v29, 4, v29\n"
  "v_mul_u32_u24 v29, 3, v29\n"
  "v_xor_b32 v25, v25, v29\n"
  "v_mul_u32_u24 v29, 1280, v25\n"
  "v_lshl_add_u32 v29, v24, 4, v29\n"
  "s_add_u32 s70, s84, 256\n"
  "v_add_u32 v24, s70, v0\n"
  "v_mul_u32_u24 v25, 6554, v24\n"
  "v_lshrrev_b32 v25, 16, v25\n"
  "v_mul_u32_u24 v30, 10, v25\n"
  "v_sub_u32 v24, v24, v30\n"
  "v_lshrrev_b32 v30, 1, v25\n"
  "v_xor_b32 v30, v30, v25\n"
  "v_and_b32 v30, 4, v30\n"
  "v_mul_u32_u24 v30, 3, v30\n"
  "v_xor_b32 v25, v25, v30\n"
  "v_mul_u32_u24 v30, 1280, v25\n"
  "v_lshl_add_u32 v30, v24, 4, v30\n"
  "s_lshl_b32 s70, s84, 4\n"
  "s_add_u32 m0, s70, 133120\n"
  "s_add_u32 s71, s82, 0\n"
  "buffer_load_dwordx4 v30, s[48:51], s71 offen nt lds\n"
  "s_lshl_b32 s70, s84, 4\n"
  "s_add_u32 m0, s70, 134144\n"
  "s_add_u32 s71, s82, 40960\n"
  "buffer_load_dwordx4 v26, s[48:51], s71 offen nt lds\n"
  "s_lshl_b32 s70, s84, 4\n"
  "s_add_u32 m0, s70, 135168\n"
  "s_add_u32 s71, s82, 40960\n"
  "buffer_load_dwordx4 v27, s[48:51], s71 offen nt lds\n"
  "s_lshl_b32 s70, s84, 4\n"
  "s_add_u32 m0, s70, 136192\n"
  "s_add_u32 s71, s82, 40960\n"
  "s_mov_b32 exec_lo, 0xffffffff\n"
  "s_mov_b32 exec_hi, 0x0\n"
  "buffer_load_dwordx4 v28, s[48:51], s71 offen nt lds\n"
  "s_mov_b64 exec, -1\n"
  "v_lshrrev_b32 v54, 5, v0\n"
  "v_lshlrev_b32 v47, 10, v54\n"
  "v_and_b32 v48, 31, v0\n"
  "v_lshl_add_u32 v46, v48, 4, v47\n"
  "v_add_u32 v47, 0x200, v47\n"
  "s_lshl_b32 s70, s33, 1\n"
  "v_add_u32 v54, s70, v54\n"
  "s_lshl_b32 s71, s34, 3\n"
  "s_add_u32 s70, s70, s71\n"
  "s_lshl_b32 s70, s70, 10\n"
  "s_add_u32 s88, s70, 1179648\n"
  "v_lshlrev_b32 v49, 3, v54\n"
  "v_add_u32 v49, 162432, v49\n"
  "v_mul_u32_u24 v54, 400, v54\n"
  "v_lshl_add_u32 v48, v48, 2, v54\n"
  "v_add_u32 v48, 30720, v48\n"
  "v_and_b32 v50, 7, v0\n"
  "v_mul_u32_u24 v50, 400, v50\n"
  "v_and_b32 v54, 0x30, v0\n"
  "v_add_u32 v50, v50, v54\n"
  "v_add_u32 v50, 30720, v50\n"
  "v_and_b32 v54, 3, v0\n"
  "v_and_b32 v52, 16, v0\n"
  "v_lshrrev_b32 v52, 2, v52\n"
  "v_add_u32 v52, v52, v54\n"
  "v_lshlrev_b32 v52, 3, v52\n"
  "v_add_u32 v52, 162432, v52\n"
  "v_and_b32 v53, 15, v0\n"
  "v_mul_u32_u24 v53, 43, v53\n"
  "v_lshrrev_b32 v53, 8, v53\n"
  "v_lshrrev_b32 v54, 4, v0\n"
  "v_mul_u32_u24 v54, 3, v54\n"
  "s_mul_i32 s71, s33, 6\n"
  "v_add3_u32 v53, v53, v54, s71\n"
  "v_lshrrev_b32 v54, 2, v53\n"
  "v_and_b32 v53, 3, v53\n"
  "v_lshlrev_b32 v53, 4, v53\n"
  "v_lshl_add_u32 v53, v54, 11, v53\n"
  "s_lshl_b32 s70, s35, 7\n"
  "s_add_u32 s70, s70, s34\n"
  "s_lshl_b32 s70, s70, 6\n"
  "s_add_u32 s76, s70, 983040\n"
  "v_lshrrev_b32 v51, 2, v0\n"
  "v_and_b32 v51, 7, v51\n"
  "v_and_b32 v54, 32, v0\n"
  "v_lshrrev_b32 v54, 1, v54\n"
  "v_add_u32 v51, v51, v54\n"
  "v_mul_u32_u24 v51, 160, v51\n"
  "v_and_b32 v54, 3, v0\n"
  "v_lshl_add_u32 v51, v54, 3, v51\n"
  "s_lshl_b32 s70, s33, 6\n"
  "s_add_u32 s70, s70, 129024\n"
  "v_add_u32 v51, s70, v51\n"
  "LCB4:\n"
  "s_lshl_b32 s99, s33, 3\n"
  "s_lshr_b32 s98, 0x1d1d1d1d, s99\n"
  "s_and_b32 s98, s98, 0xff\n"
  "Ldl_21:\n"
  "s_sub_u32 s98, s98, 1\n"
  "s_cbranch_scc0 Ldl_21\n"
  "buffer_load_dwordx4 v[6:9], v46, s[56:59], s88 offen sc1\n"
  "LPB4_22:\n"
  "s_waitcnt vmcnt(0)\n"
  "v_cmp_ne_u32 vcc, s62, v9\n"
  "s_nop 0\n"
  "s_or_b64 s[64:65], vcc, vcc\n"
  "s_cbranch_scc1 LSB4_23\n"
  "ds_write2_b32 v48, v6, v7 offset0:0 offset1:32\n"
  "ds_write_b32 v48, v8 offset:256\n"
  "s_waitcnt lgkmcnt(0)\n"
  "s_barrier\n"
  "s_cmp_eq_u32 s33, 3\n"
  "s_cbranch_scc1 Lsk_24\n"
  "ds_read_b128 v[6:9], v50 offset:0\n"
  "ds_read_b64_tr_b16 v[18:19], v51 offset:0\n"
  "ds_read_b64_tr_b16 v[20:21], v51 offset:1280\n"
  "ds_read_b64_tr_b16 v[22:23], v51 offset:32\n"
  "ds_read_b64_tr_b16 v[24:25], v51 offset:1312\n"
  "ds_read_b128 v[10:13], v50 offset:64\n"
  "ds_read_b64_tr_b16 v[26:27], v51 offset:5120\n"
  "ds_read_b64_tr_b16 v[28:29], v51 offset:6400\n"
  "ds_read_b64_tr_b16 v[30:31], v51 offset:5152\n"
  "ds_read_b64_tr_b16 v[32:33], v51 offset:6432\n"
  "s_waitcnt lgkmcnt(5)\n"
  "ds_read_b128 v[14:17], v50 offset:128\n"
  "ds_read_b64_tr_b16 v[34:35], v51 offset:10240\n"
  "ds_read_b64_tr_b16 v[36:37], v51 offset:11520\n"
  "ds_read_b64_tr_b16 v[38:39], v51 offset:10272\n"
  "ds_read_b64_tr_b16 v[40:41], v51 offset:11552\n"
  "v_mfma_f32_16x16x32_bf16 v[42:45], v[6:9], v[18:21], 0\n"
  "v_mfma_f32_16x16x32_bf16 v[2:5], v[6:9], v[22:25], 0\n"
  "s_waitcnt lgkmcnt(5)\n"
  "ds_read_b128 v[6:9], v50 offset:192\n"
  "ds_read_b64_tr_b16 v[18:19], v51 offset:15360\n"
  "ds_read_b64_tr_b16 v[20:21], v51 offset:16640\n"
  "ds_read_b64_tr_b16 v[22:23], v51 offset:15392\n"
  "ds_read_b64_tr_b16 v[24:25], v51 offset:16672\n"
  "v_mfma_f32_16x16x32_bf16 v[42:45], v[10:13], v[26:29], v[42:45]\n"
  "v_mfma_f32_16x16x32_bf16 v[2:5], v[10:13], v[30:33], v[2:5]\n"
  "s_waitcnt lgkmcnt(5)\n"
  "ds_read_b128 v[10:13], v50 offset:256\n"
  "ds_read_b64_tr_b16 v[26:27], v51 offset:20480\n"
  "ds_read_b64_tr_b16 v[28:29], v51 offset:21760\n"
  "ds_read_b64_tr_b16 v[30:31], v51 offset:20512\n"
  "ds_read_b64_tr_b16 v[32:33], v51 offset:21792\n"
  "v_mfma_f32_16x16x32_bf16 v[42:45], v[14:17], v[34:37], v[42:45]\n"
  "v_mfma_f32_16x16x32_bf16 v[2:5], v[14:17], v[38:41], v[2:5]\n"
  "s_waitcnt lgkmcnt(5)\n"
  "ds_read_b128 v[14:17], v50 offset:320\n"
  "ds_read_b64_tr_b16 v[34:35], v51 offset:25600\n"
  "ds_read_b64_tr_b16 v[36:37], v51 offset:26880\n"
  "ds_read_b64_tr_b16 v[38:39], v51 offset:25632\n"
  "ds_read_b64_tr_b16 v[40:41], v51 offset:26912\n"
  "v_mfma_f32_16x16x32_bf16 v[42:45], v[6:9], v[18:21], v[42:45]\n"
  "v_mfma_f32_16x16x32_bf16 v[2:5], v[6:9], v[22:25], v[2:5]\n"
  "s_waitcnt lgkmcnt(5)\n"
  "v_mfma_f32_16x16x32_bf16 v[42:45], v[10:13], v[26:29], v[42:45]\n"
  "v_mfma_f32_16x16x32_bf16 v[2:5], v[10:13], v[30:33], v[2:5]\n"
  "s_waitcnt lgkmcnt(0)\n"
  "v_mfma_f32_16x16x32_bf16 v[42:45], v[14:17], v[34:37], v[42:45]\n"
  "v_mfma_f32_16x16x32_bf16 v[2:5], v[14:17], v[38:41], v[2:5]\n"
  "v_mov_b32 v9, s62\n"
  "s_cmp_eq_u32 s33, 2\n"
  "s_mov_b32 s71, 0x1041\n"
  "s_cselect_b32 s70, s71, 0x10411041\n"
  "s_nop 2\n"
  "v_add_f32 v6, v42, v43\n"
  "v_add_f32 v10, v44, v45\n"
  "v_add_f32 v7, v2, v3\n"
  "v_add_f32 v11, v4, v5\n"
  "v_add_f32 v6, v6, v10\n"
  "v_add_f32 v7, v7, v11\n"
  "s_nop 1\n"
  "v_permlane16_swap_b32 v6, v7\n"
  "v_add_f32 v6, v6, v7\n"
  "s_nop 1\n"
  "v_mov_b32_dpp v7, v6 row_shl:1 row_mask:0xf bank_mask:0xf bound_ctrl:0\n"
  "v_cvt_pk_bf16_f32 v6, v6, v7\n"
  "s_nop 1\n"
  "v_mov_b32_dpp v7, v6 row_shl:2 row_mask:0xf bank_mask:0xf bound_ctrl:0\n"
  "v_mov_b32_dpp v8, v6 row_shl:4 row_mask:0xf bank_mask:0xf bound_ctrl:0\n"
  "s_mov_b32 exec_lo, s70\n"
  "s_mov_b32 exec_hi, 0\n"
  "buffer_store_dwordx4 v[6:9], v53, s[56:59], s76 offen sc1\n"
  "s_mov_b64 exec, -1\n"
  "Lsk_24:\n"
  "s_add_u32 s70, s84, 0\n"
  "v_add_u32 v24, s70, v0\n"
  "v_mul_u32_u24 v25, 2731, v24\n"
  "v_lshrrev_b32 v25, 16, v25\n"
  "v_mul_u32_u24 v26, 24, v25\n"
  "v_sub_u32 v24, v24, v26\n"
  "v_and_b32 v26, 6, v25\n"
  "v_xor_b32 v24, v24, v26\n"
  "v_mul_u32_u24 v26, 12288, v25\n"
  "v_lshl_add_u32 v26, v24, 4, v26\n"
  "s_add_u32 s70, s84, 64\n"
  "v_add_u32 v24, s70, v0\n"
  "v_mul_u32_u24 v25, 2731, v24\n"
  "v_lshrrev_b32 v25, 16, v25\n"
  "v_mul_u32_u24 v27, 24, v25\n"
  "v_sub_u32 v24, v24, v27\n"
  "v_and_b32 v27, 6, v25\n"
  "v_xor_b32 v24, v24, v27\n"
  "v_mul_u32_u24 v27, 12288, v25\n"
  "v_lshl_add_u32 v27, v24, 4, v27\n"
  "s_add_u32 s70, s84, 128\n"
  "v_add_u32 v24, s70, v0\n"
  "v_mul_u32_u24 v25, 2731, v24\n"
  "v_lshrrev_b32 v25, 16, v25\n"
  "v_mul_u32_u24 v28, 24, v25\n"
  "v_sub_u32 v24, v24, v28\n"
  "v_and_b32 v28, 6, v25\n"
  "v_xor_b32 v24, v24, v28\n"
  "v_mul_u32_u24 v28, 12288, v25\n"
  "v_lshl_add_u32 v28, v24, 4, v28\n"
  "s_lshl_b32 s70, s84, 4\n"
  "s_add_u32 m0, s70, 0\n"
  "s_add_u32 s71, s81, 0\n"
  "buffer_load_dwordx4 v26, s[20:23], s71 offen  lds\n"
  "s_lshl_b32 s70, s84, 4\n"
  "s_add_u32 m0, s70, 1024\n"
  "s_add_u32 s71, s81, 0\n"
  "buffer_load_dwordx4 v27, s[20:23], s71 offen  lds\n"
  "s_lshl_b32 s70, s84, 4\n"
  "s_add_u32 m0, s70, 2048\n"
  "s_add_u32 s71, s81, 0\n"
  "buffer_load_dwordx4 v28, s[20:23], s71 offen  lds\n"
  "s_lshl_b32 s70, s84, 4\n"
  "s_add_u32 m0, s70, 3072\n"
  "s_add_u32 s71, s81, 98304\n"
  "buffer_load_dwordx4 v26, s[20:23], s71 offen  lds\n"
  "s_lshl_b32 s70, s84, 4\n"
  "s_add_u32 m0, s70, 4096\n"
  "s_add_u32 s71, s81, 98304\n"
  "buffer_load_dwordx4 v27, s[20:23], s71 offen  lds\n"
  "s_lshl_b32 s70, s84, 4\n"
  "s_add_u32 m0, s70, 5120\n"
  "s_add_u32 s71, s81, 98304\n"
  "buffer_load_dwordx4 v28, s[20:23], s71 offen  lds\n"
  "s_lshl_b32 s70, s84, 4\n"
  "s_add_u32 m0, s70, 6144\n"
  "s_add_u32 s71, s81, 196608\n"
  "buffer_load_dwordx4 v26, s[20:23], s71 offen  lds\n"
  "s_lshl_b32 s70, s84, 4\n"
  "s_add_u32 m0, s70, 7168\n"
  "s_add_u32 s71, s81, 196608\n"
  "s_mov_b32 exec_lo, 0xffffffff\n"
  "s_mov_b32 exec_hi, 0x0\n"
  "buffer_load_dwordx4 v27, s[20:23], s71 offen  lds\n"
  "s_mov_b64 exec, -1\n"
  "s_lshl_b32 s70, s33, 2\n"
  "v_and_b32 v2, 3, v0\n"
  "v_add_u32 v2, s70, v2\n"
  "v_min_u32 v3, 14, v2\n"
  "v_sub_u32 v18, v3, v2\n"
  "v_add_u32 v18, v18, v0\n"
  "v_lshlrev_b32 v18, 4, v18\n"
  "s_lshl_b32 s70, s35, 2\n"
  "s_add_u32 s70, s70, s33\n"
  "s_lshl_b32 s70, s70, 11\n"
  "s_add_u32 s88, s70, 983040\n"
  "v_lshrrev_b32 v11, 2, v0\n"
  "v_mul_u32_u24 v10, 384, v11\n"
  "v_and_b32 v11, 6, v11\n"
  "v_lshl_add_u32 v10, v11, 4, v10\n"
  "v_and_b32 v11, 3, v0\n"
  "v_lshl_add_u32 v10, v11, 3, v10\n"
  "s_mul_i32 s70, s33, 3\n"
  "s_add_u32 s70, s70, 0\n"
  "s_and_b32 s71, s70, 3\n"
  "s_lshl_b32 s71, s71, 5\n"
  "s_lshr_b32 s70, s70, 2\n"
  "s_lshl_b32 s70, s70, 7\n"
  "v_xor_b32 v6, s71, v10\n"
  "v_add_u32 v6, s70, v6\n"
  "s_mul_i32 s70, s33, 3\n"
  "s_add_u32 s70, s70, 1\n"
  "s_and_b32 s71, s70, 3\n"
  "s_lshl_b32 s71, s71, 5\n"
  "s_lshr_b32 s70, s70, 2\n"
  "s_lshl_b32 s70, s70, 7\n"
  "v_xor_b32 v7, s71, v10\n"
  "v_add_u32 v7, s70, v7\n"
  "s_mul_i32 s70, s33, 3\n"
  "s_add_u32 s70, s70, 2\n"
  "s_and_b32 s71, s70, 3\n"
  "s_lshl_b32 s71, s71, 5\n"
  "s_lshr_b32 s70, s70, 2\n"
  "s_lshl_b32 s70, s70, 7\n"
  "v_xor_b32 v8, s71, v10\n"
  "v_add_u32 v8, s70, v8\n"
  "s_waitcnt vmcnt(0)\n"
  "s_waitcnt lgkmcnt(0)\n"
  "s_barrier\n"
  "ds_read_b64_tr_b16 v[24:25], v6 offset:0\n"
  "ds_read_b64_tr_b16 v[26:27], v6 offset:6144\n"
  "ds_read_b64_tr_b16 v[28:29], v6 offset:12288\n"
  "ds_read_b64_tr_b16 v[30:31], v6 offset:18432\n"
  "ds_read_b64_tr_b16 v[32:33], v6 offset:24576\n"
  "ds_read_b64_tr_b16 v[34:35], v7 offset:0\n"
  "ds_read_b64_tr_b16 v[36:37], v7 offset:6144\n"
  "ds_read_b64_tr_b16 v[38:39], v7 offset:12288\n"
  "ds_read_b64_tr_b16 v[40:41], v7 offset:18432\n"
  "ds_read_b64_tr_b16 v[42:43], v7 offset:24576\n"
  "ds_read_b64_tr_b16 v[44:45], v8 offset:0\n"
  "ds_read_b64_tr_b16 v[46:47], v8 offset:6144\n"
  "ds_read_b64_tr_b16 v[48:49], v8 offset:12288\n"
  "ds_read_b64_tr_b16 v[50:51], v8 offset:18432\n"
  "ds_read_b64_tr_b16 v[52:53], v8 offset:24576\n"
  "s_waitcnt lgkmcnt(0)\n"
  "LCB5:\n"
  "s_lshl_b32 s99, s33, 3\n"
  "s_lshr_b32 s98, 0x2040404, s99\n"
  "s_and_b32 s98, s98, 0xff\n"
  "Ldl_25:\n"
  "s_sub_u32 s98, s98, 1\n"
  "s_cbranch_scc0 Ldl_25\n"
  "buffer_load_dwordx4 v[6:9], v18, s[56:59], s88 offen offset:0 sc1\n"
  "buffer_load_dwordx4 v[10:13], v18, s[56:59], s88 offen offset:1024 sc1\n"
  "v_mul_u32_u24 v4, 86, v3\n"
  "v_lshrrev_b32 v4, 8, v4\n"
  "v_mul_u32_u24 v5, 3, v4\n"
  "v_sub_u32 v3, v3, v5\n"
  "v_mul_u32_u24 v3, 3, v3\n"
  "v_lshrrev_b32 v5, 4, v0\n"
  "v_add_u32 v3, v3, v5\n"
  "v_cmp_gt_u32 vcc, 8, v3\n"
  "v_cmp_gt_u32_e64 s[66:67], 15, v2\n"
  "s_nop 0\n"
  "s_and_b64 s[68:69], vcc, s[66:67]\n"
  "s_and_b32 s68, s68, 0x000f000f\n"
  "s_and_b32 s69, s69, 0x000f\n"
  "v_lshl_add_u32 v3, v4, 3, v3\n"
  "v_lshlrev_b32 v3, 2, v3\n"
  "v_add_u32 v19, 160320, v3\n"
  "v_mov_b32 v4, 160480\n"
  "v_cndmask_b32_e64 v19, v4, v19, s[68:69]\n"
  "ds_read_b32 v21, v19\n"
  "v_and_b32 v4, 1, v0\n"
  "v_lshlrev_b32 v4, 4, v4\n"
  "v_mov_b32 v5, 0x3f80\n"
  "v_lshlrev_b32 v4, v4, v5\n"
  "v_mov_b32 v17, 0\n"
  "s_mov_b32 s66, 0x30003\n"
  "s_mov_b32 s67, 0x30003\n"
  "v_cndmask_b32_e64 v14, 0, v4, s[66:67]\n"
  "s_mov_b32 s66, 0x300030\n"
  "s_mov_b32 s67, 0x300030\n"
  "v_cndmask_b32_e64 v15, 0, v4, s[66:67]\n"
  "s_mov_b32 s66, 0x3000300\n"
  "s_mov_b32 s67, 0x3000300\n"
  "v_cndmask_b32_e64 v16, 0, v4, s[66:67]\n"
  "s_waitcnt lgkmcnt(0)\n"
  "v_lshlrev_b32 v20, 16, v21\n"
  "v_and_b32 v21, 0xffff0000, v21\n"
  "s_mov_b32 s66, 0x02100210\n"
  "s_mov_b32 s67, 0x02100210\n"
  "v_lshrrev_b32 v2, 4, v0\n"
  "v_lshlrev_b32 v2, 3, v2\n"
  "v_add_u32 v22, 160320, v2\n"
  "v_add_u32 v2, 160128, v2\n"
  "v_cndmask_b32_e64 v22, v22, v2, s[66:67]\n"
  "s_lshl_b32 s70, s34, 5\n"
  "s_lshl_b32 s71, s33, 3\n"
  "s_add_u32 s70, s70, s71\n"
  "s_add_u32 s70, s70, s35\n"
  "s_lshl_b32 s71, s70, 4\n"
  "s_lshl_b32 s70, s70, 8\n"
  "s_add_u32 s71, s71, 589824\n"
  "v_lshlrev_b32 v54, 4, v0\n"
  "v_add_u32 v54, s70, v54\n"
  "v_add_u32 v2, 327424, v54\n"
  "v_add_u32 v54, 65536, v54\n"
  "s_mov_b32 s66, 0xffff0000\n"
  "s_mov_b32 s67, 0\n"
  "v_cndmask_b32_e64 v54, v54, v2, s[66:67]\n"
  "v_mov_b32 v2, s71\n"
  "s_mov_b32 s66, 0\n"
  "s_mov_b32 s67, -1\n"
  "v_cndmask_b32_e64 v54, v54, v2, s[66:67]\n"
  "LPB5_26:\n"
  "s_waitcnt vmcnt(1)\n"
  "v_mfma_f32_16x16x32_bf16 v[2:5], v[14:17], v[6:9], 0\n"
  "v_cmp_ne_u32_e64 s[64:65], s62, v9\n"
  "s_waitcnt vmcnt(0)\n"
  "v_mfma_f32_16x16x32_bf16 v[2:5], v[14:17], v[10:13], v[2:5]\n"
  "v_cmp_ne_u32 vcc, s62, v13\n"
  "s_nop 0\n"
  "s_or_b64 s[64:65], s[64:65], vcc\n"
  "s_cbranch_scc1 LSB5_27\n"
  "s_nop 3\n"
  "v_add_f32_dpp v2, v2, v2 row_ror:4 row_mask:0xf bank_mask:0xf\n"
  "v_add_f32_dpp v3, v3, v3 row_ror:4 row_mask:0xf bank_mask:0xf\n"
  "s_nop 0\n"
  "v_add_f32_dpp v2, v2, v2 row_ror:8 row_mask:0xf bank_mask:0xf\n"
  "v_add_f32_dpp v3, v3, v3 row_ror:8 row_mask:0xf bank_mask:0xf\n"
  "v_cvt_pk_bf16_f32 v3, v2, v3\n"
  "v_lshlrev_b32 v2, 16, v3\n"
  "v_and_b32 v3, 0xffff0000, v3\n"
  "v_add_f32 v2, v20, v2\n"
  "v_add_f32 v3, v21, v3\n"
  "v_cvt_pk_bf16_f32 v2, v2, v3\n"
  "ds_write_b32 v19, v2\n"
  "s_add_u32 s62, s62, 1\n"
  "s_waitcnt lgkmcnt(0)\n"
  "s_barrier\n"
  "ds_read2_b64 v[6:9], v22 offset0:0 offset1:4\n"
  "ds_read2_b64 v[10:13], v22 offset0:8 offset1:12\n"
  "ds_read_b64 v[4:5], v22 offset:128\n"
  "v_mov_b32 v2, 0\n"
  "v_mov_b32 v3, 0\n"
  "s_mov_b32 s66, 0\n"
  "s_mov_b32 s67, -1\n"
  "s_waitcnt lgkmcnt(2)\n"
  "v_mfma_f32_16x16x32_bf16 v[18:21], v[6:9], v[24:27], 0\n"
  "v_mfma_f32_16x16x32_bf16 v[14:17], v[6:9], v[6:9], 0\n"
  "s_waitcnt lgkmcnt(1)\n"
  "v_mfma_f32_16x16x32_bf16 v[18:21], v[10:13], v[28:31], v[18:21]\n"
  "v_mfma_f32_16x16x32_bf16 v[14:17], v[10:13], v[10:13], v[14:17]\n"
  "s_waitcnt lgkmcnt(0)\n"
  "v_mfma_f32_16x16x32_bf16 v[18:21], v[2:5], v[30:33], v[18:21]\n"
  "v_mfma_f32_16x16x32_bf16 v[14:17], v[2:5], v[2:5], v[14:17]\n"
  "v_mfma_f32_16x16x32_bf16 v[24:27], v[6:9], v[34:37], 0\n"
  "v_mfma_f32_16x16x32_bf16 v[24:27], v[10:13], v[38:41], v[24:27]\n"
  "v_mfma_f32_16x16x32_bf16 v[24:27], v[2:5], v[40:43], v[24:27]\n"
  "v_mfma_f32_16x16x32_bf16 v[30:33], v[6:9], v[44:47], 0\n"
  "v_mfma_f32_16x16x32_bf16 v[30:33], v[10:13], v[48:51], v[30:33]\n"
  "v_mfma_f32_16x16x32_bf16 v[30:33], v[2:5], v[50:53], v[30:33]\n"
  "s_nop 1\n"
  "v_cndmask_b32_e64 v28, v18, v14, s[66:67]\n"
  "s_nop 1\n"
  "v_cndmask_b32_e64 v29, v24, v15, s[66:67]\n"
  "s_nop 1\n"
  "v_mov_b32 v31, s62\n"
  "s_mov_b32 exec_lo, 0xffff\n"
  "s_mov_b32 exec_hi, 0x1\n"
  "buffer_store_dwordx4 v[28:31], v54, s[56:59], 0 offen sc1\n"
  "s_mov_b64 exec, -1\n"
  "v_lshlrev_b32 v26, 4, v0\n"
  "v_lshrrev_b32 v27, 1, v0\n"
  "v_and_b32 v27, 7, v27\n"
  "v_lshlrev_b32 v27, 4, v27\n"
  "s_lshl_b32 s70, s34, 2\n"
  "s_add_u32 s70, s70, s33\n"
  "s_lshl_b32 s71, s70, 7\n"
  "s_lshl_b32 s70, s70, 11\n"
  "s_add_u32 s88, s71, 589824\n"
  "s_add_u32 s89, s70, 65536\n"
  "s_add_u32 s90, s70, 327680\n"
  "v_lshrrev_b32 v28, 4, v0\n"
  "v_lshlrev_b32 v28, 1, v28\n"
  "v_mov_b32 v29, 0x98\n"
  "v_lshrrev_b32 v28, v28, v29\n"
  "v_and_b32 v28, 3, v28\n"
  "v_and_b32 v29, 15, v0\n"
  "v_lshl_add_u32 v28, v28, 4, v29\n"
  "s_mul_i32 s70, s33, 48\n"
  "v_add_u32 v28, s70, v28\n"
  "v_lshlrev_b32 v29, 2, v28\n"
  "v_add_u32 v29, 160896, v29\n"
  "v_and_b32 v30, 0xe1, v28\n"
  "v_and_b32 v6, 0x1c, v28\n"
  "v_lshl_add_u32 v30, v30, 1, v6\n"
  "v_and_b32 v28, 2, v28\n"
  "v_lshl_add_u32 v28, v28, 4, v30\n"
  "v_add_u32 v28, 160512, v28\n"
  "ds_read_b32 v18, v29\n"
  "s_waitcnt lgkmcnt(0)\n"
  "v_and_b32 v6, 3, v0\n"
  "v_and_b32 v7, 16, v0\n"
  "v_lshl_add_u32 v6, v6, 5, v7\n"
  "v_add_u32 v6, 160512, v6\n"
  "v_mov_b32 v8, 159744\n"
  "v_and_b32 v31, 1, v0\n"
  "v_mul_u32_u24 v31, 0xaaaa, v31\n"
  "v_add_u32 v31, 0x4444, v31\n"
  "s_mov_b32 s66, 0xf000f\n"
  "s_mov_b32 s67, 0xf000f0\n"
  "v_cndmask_b32_e64 v48, v8, v6, s[66:67]\n"
  "s_mov_b32 s66, 0xf000f00\n"
  "s_mov_b32 s67, 0xf000f000\n"
  "v_cndmask_b32_e64 v49, v8, v6, s[66:67]\n"
  "v_mov_b32 v32, 0\n"
  "v_mov_b32 v33, 0\n"
  "v_mov_b32 v34, 0\n"
  "v_mov_b32 v35, 0\n"
  "v_mov_b32 v36, 0\n"
  "v_mov_b32 v37, 0\n"
  "v_mov_b32 v38, 0\n"
  "v_mov_b32 v39, 0\n"
  "v_mov_b32 v40, 0\n"
  "v_mov_b32 v41, 0\n"
  "v_mov_b32 v42, 0\n"
  "v_mov_b32 v43, 0\n"
  "v_mov_b32 v44, 0\n"
  "v_mov_b32 v45, 0\n"
  "v_mov_b32 v46, 0\n"
  "v_mov_b32 v47, 0\n"
  "s_lshl_b32 s70, s33, 8\n"
  "s_add_u32 s70, s70, 162560\n"
  "v_and_b32 v6, 32, v0\n"
  "v_lshl_add_u32 v6, v6, 1, v7\n"
  "v_and_b32 v7, 1, v0\n"
  "v_lshl_add_u32 v6, v7, 5, v6\n"
  "v_add_u32 v6, s70, v6\n"
  "s_mov_b32 s66, 0xf000f\n"
  "s_mov_b32 s67, 0xf000f\n"
  "v_cndmask_b32_e64 v51, v8, v6, s[66:67]\n"
  "s_mov_b32 s66, 0xf000f0\n"
  "s_mov_b32 s67, 0xf000f0\n"
  "v_cndmask_b32_e64 v52, v8, v6, s[66:67]\n"
  "s_mov_b32 s66, 0xf000f00\n"
  "s_mov_b32 s67, 0xf000f00\n"
  "v_cndmask_b32_e64 v53, v8, v6, s[66:67]\n"
  "s_mov_b32 s66, 0xf000f000\n"
  "s_mov_b32 s67, 0xf000f000\n"
  "v_cndmask_b32_e64 v54, v8, v6, s[66:67]\n"
  "v_and_b32 v50, 0x2d, v0\n"
  "v_lshlrev_b32 v50, 1, v50\n"
  "v_and_b32 v6, 2, v0\n"
  "v_lshl_add_u32 v50, v6, 4, v50\n"
  "v_and_b32 v6, 16, v0\n"
  "v_lshrrev_b32 v6, 2, v6\n"
  "v_add_u32 v50, v50, v6\n"
  "v_add_u32 v50, s70, v50\n"
  "v_add_u32 v24, 0, v0\n"
  "v_mul_u32_u24 v25, 2731, v24\n"
  "v_lshrrev_b32 v25, 16, v25\n"
  "v_mul_u32_u24 v1, 24, v25\n"
  "v_sub_u32 v24, v24, v1\n"
  "v_and_b32 v1, 6, v25\n"
  "v_xor_b32 v24, v24, v1\n"
  "v_mul_u32_u24 v1, 1536, v25\n"
  "v_lshl_add_u32 v1, v24, 4, v1\n"
  "v_add_u32 v24, 64, v0\n"
  "v_mul_u32_u24 v25, 2731, v24\n"
  "v_lshrrev_b32 v25, 16, v25\n"
  "v_mul_u32_u24 v2, 24, v25\n"
  "v_sub_u32 v24, v24, v2\n"
  "v_and_b32 v2, 6, v25\n"
  "v_xor_b32 v24, v24, v2\n"
  "v_mul_u32_u24 v2, 1536, v25\n"
  "v_lshl_add_u32 v2, v24, 4, v2\n"
  "v_add_u32 v24, 128, v0\n"
  "v_mul_u32_u24 v25, 2731, v24\n"
  "v_lshrrev_b32 v25, 16, v25\n"
  "v_mul_u32_u24 v3, 24, v25\n"
  "v_sub_u32 v24, v24, v3\n"
  "v_and_b32 v3, 6, v25\n"
  "v_xor_b32 v24, v24, v3\n"
  "v_mul_u32_u24 v3, 1536, v25\n"
  "v_lshl_add_u32 v3, v24, 4, v3\n"
  "v_add_u32 v4, 12288, v1\n"
  "s_cmp_eq_u32 s33, 0\n"
  "s_cselect_b64 vcc, -1, 0\n"
  "v_cndmask_b32 v4, v4, v1, vcc\n"
  "s_cmp_eq_u32 s33, 1\n"
  "s_cselect_b64 vcc, -1, 0\n"
  "v_cndmask_b32 v4, v4, v2, vcc\n"
  "s_cmp_eq_u32 s33, 2\n"
  "s_cselect_b64 vcc, -1, 0\n"
  "v_cndmask_b32 v4, v4, v3, vcc\n"
  "s_add_u32 s70, s84, 0\n"
  "v_add_u32 v24, s70, v0\n"
  "v_mul_u32_u24 v25, 6554, v24\n"
  "v_lshrrev_b32 v25, 16, v25\n"
  "v_mul_u32_u24 v19, 10, v25\n"
  "v_sub_u32 v24, v24, v19\n"
  "v_lshrrev_b32 v19, 1, v25\n"
  "v_xor_b32 v19, v19, v25\n"
  "v_and_b32 v19, 4, v19\n"
  "v_mul_u32_u24 v19, 3, v19\n"
  "v_xor_b32 v25, v25, v19\n"
  "v_mul_u32_u24 v19, 1280, v25\n"
  "v_lshl_add_u32 v19, v24, 4, v19\n"
  "s_add_u32 s70, s84, 64\n"
  "v_add_u32 v24, s70, v0\n"
  "v_mul_u32_u24 v25, 6554, v24\n"
  "v_lshrrev_b32 v25, 16, v25\n"
  "v_mul_u32_u24 v20, 10, v25\n"
  "v_sub_u32 v24, v24, v20\n"
  "v_lshrrev_b32 v20, 1, v25\n"
  "v_xor_b32 v20, v20, v25\n"
  "v_and_b32 v20, 4, v20\n"
  "v_mul_u32_u24 v20, 3, v20\n"
  "v_xor_b32 v25, v25, v20\n"
  "v_mul_u32_u24 v20, 1280, v25\n"
  "v_lshl_add_u32 v20, v24, 4, v20\n"
  "s_add_u32 s70, s84, 128\n"
  "v_add_u32 v24, s70, v0\n"
  "v_mul_u32_u24 v25, 6554, v24\n"
  "v_lshrrev_b32 v25, 16, v25\n"
  "v_mul_u32_u24 v21, 10, v25\n"
  "v_sub_u32 v24, v24, v21\n"
  "v_lshrrev_b32 v21, 1, v25\n"
  "v_xor_b32 v21, v21, v25\n"
  "v_and_b32 v21, 4, v21\n"
  "v_mul_u32_u24 v21, 3, v21\n"
  "v_xor_b32 v25, v25, v21\n"
  "v_mul_u32_u24 v21, 1280, v25\n"
  "v_lshl_add_u32 v21, v24, 4, v21\n"
  "s_add_u32 s70, s84, 192\n"
  "v_add_u32 v24, s70, v0\n"
  "v_mul_u32_u24 v25, 6554, v24\n"
  "v_lshrrev_b32 v25, 16, v25\n"
  "v_mul_u32_u24 v22, 10, v25\n"
  "v_sub_u32 v24, v24, v22\n"
  "v_lshrrev_b32 v22, 1, v25\n"
  "v_xor_b32 v22, v22, v25\n"
  "v_and_b32 v22, 4, v22\n"
  "v_mul_u32_u24 v22, 3, v22\n"
  "v_xor_b32 v25, v25, v22\n"
  "v_mul_u32_u24 v22, 1280, v25\n"
  "v_lshl_add_u32 v22, v24, 4, v22\n"
  "s_add_u32 s70, s84, 256\n"
  "v_add_u32 v24, s70, v0\n"
  "v_mul_u32_u24 v25, 6554, v24\n"
  "v_lshrrev_b32 v25, 16, v25\n"
  "v_mul_u32_u24 v23, 10, v25\n"
  "v_sub_u32 v24, v24, v23\n"
  "v_lshrrev_b32 v23, 1, v25\n"
  "v_xor_b32 v23, v23, v25\n"
  "v_and_b32 v23, 4, v23\n"
  "v_mul_u32_u24 v23, 3, v23\n"
  "v_xor_b32 v25, v25, v23\n"
  "v_mul_u32_u24 v23, 1280, v25\n"
  "v_lshl_add_u32 v23, v24, 4, v23\n"
  "LCA1x:\n"
  "s_lshl_b32 s99, s33, 3\n"
  "s_lshr_b32 s98, 0xd0d0d0d, s99\n"
  "s_and_b32 s98, s98, 0xff\n"
  "Ldl_28:\n"
  "s_sub_u32 s98, s98, 1\n"
  "s_cbranch_scc0 Ldl_28\n"
  "buffer_load_dwordx4 v[6:9], v27, s[56:59], s88 offen sc1\n"
  "buffer_load_dwordx4 v[10:13], v26, s[56:59], s89 offen sc1\n"
  "buffer_load_dwordx4 v[14:17], v26, s[56:59], s89 offen offset:1024 sc1\n"
  "LPA1_29:\n"
  "s_waitcnt vmcnt(2)\n"
  "v_cmp_ne_u32_e64 s[64:65], s62, v9\n"
  "v_add_f32_dpp v6, v6, v6 quad_perm:[2,3,0,1] row_mask:0xf bank_mask:0xf\n"
  "v_add_f32_dpp v7, v7, v7 quad_perm:[2,3,0,1] row_mask:0xf bank_mask:0xf\n"
  "s_nop 0\n"
  "v_add_f32_dpp v6, v6, v6 row_ror:4 row_mask:0xf bank_mask:0xf\n"
  "v_add_f32_dpp v7, v7, v7 row_ror:4 row_mask:0xf bank_mask:0xf\n"
  "s_nop 0\n"
  "v_add_f32_dpp v6, v6, v6 row_ror:8 row_mask:0xf bank_mask:0xf\n"
  "v_add_f32_dpp v7, v7, v7 row_ror:8 row_mask:0xf bank_mask:0xf\n"
  "v_mul_f32 v7, 0x3acccccd, v7\n"
  "v_mul_f32 v8, v7, v7\n"
  "v_mov_b32 v30, 0x3acccccd\n"
  "v_fma_f32 v6, v6, v30, -v8\n"
  "v_max_f32 v6, 0, v6\n"
  "v_add_f32 v6, 0x3727c5ac, v6\n"
  "v_rsq_f32 v6, v6\n"
  "v_mul_f32 v7, v7, v18\n"
  "s_waitcnt vmcnt(1)\n"
  "v_cmp_ne_u32 vcc, s62, v13\n"
  "s_nop 0\n"
  "s_or_b64 s[64:65], s[64:65], vcc\n"
  "s_waitcnt vmcnt(0)\n"
  "v_cmp_ne_u32 vcc, s62, v17\n"
  "s_nop 0\n"
  "s_or_b64 s[64:65], s[64:65], vcc\n"
  "s_cbranch_scc1 LSA1_30\n"
  "s_lshl_b32 s70, s84, 4\n"
  "s_add_u32 m0, s70, 0\n"
  "s_add_u32 s71, s82, 0\n"
  "buffer_load_dwordx4 v19, s[24:27], s71 offen  lds\n"
  "s_lshl_b32 s70, s84, 4\n"
  "s_add_u32 m0, s70, 1024\n"
  "s_add_u32 s71, s82, 0\n"
  "buffer_load_dwordx4 v20, s[24:27], s71 offen  lds\n"
  "s_lshl_b32 s70, s84, 4\n"
  "s_add_u32 m0, s70, 2048\n"
  "s_add_u32 s71, s82, 0\n"
  "buffer_load_dwordx4 v21, s[24:27], s71 offen  lds\n"
  "s_lshl_b32 s70, s84, 4\n"
  "s_add_u32 m0, s70, 3072\n"
  "s_add_u32 s71, s82, 0\n"
  "buffer_load_dwordx4 v22, s[24:27], s71 offen  lds\n"
  "s_lshl_b32 s70, s84, 4\n"
  "s_add_u32 m0, s70, 4096\n"
  "s_add_u32 s71, s82, 0\n"
  "buffer_load_dwordx4 v23, s[24:27], s71 offen  lds\n"
  "s_lshl_b32 s70, s84, 4\n"
  "s_add_u32 m0, s70, 5120\n"
  "s_add_u32 s71, s82, 40960\n"
  "buffer_load_dwordx4 v19, s[24:27], s71 offen  lds\n"
  "s_lshl_b32 s70, s84, 4\n"
  "s_add_u32 m0, s70, 6144\n"
  "s_add_u32 s71, s82, 40960\n"
  "buffer_load_dwordx4 v20, s[24:27], s71 offen  lds\n"
  "s_lshl_b32 s70, s84, 4\n"
  "s_add_u32 m0, s70, 7168\n"
  "s_add_u32 s71, s82, 40960\n"
  "s_mov_b32 exec_lo, 0xffffffff\n"
  "s_mov_b32 exec_hi, 0x0\n"
  "buffer_load_dwordx4 v21, s[24:27], s71 offen  lds\n"
  "s_mov_b64 exec, -1\n"
  "v_add_f32 v10, v10, v14\n"
  "v_add_f32 v11, v11, v15\n"
  "v_add_f32 v12, v12, v16\n"
  "v_mov_b32 v30, v12\n"
  "v_permlane32_swap_b32 v10, v11\n"
  "s_nop 0\n"
  "v_permlane32_swap_b32 v12, v30\n"
  "v_add_f32 v10, v10, v11\n"
  "v_add_f32 v12, v12, v30\n"
  "s_nop 1\n"
  "v_permlane16_swap_b32 v10, v12\n"
  "v_add_f32 v10, v10, v12\n"
  "v_sub_f32 v10, v10, v7\n"
  "v_mul_f32 v10, v10, v6\n"
  "v_cvt_pk_bf16_f32 v10, v10, v10\n"
  "ds_write_b16 v28, v10\n"
  "s_waitcnt lgkmcnt(0)\n"
  "s_barrier\n"
  "ds_read_b128 v[6:9], v48 offset:0\n"
  "ds_read_b128 v[10:13], v48 offset:128\n"
  "ds_read_b128 v[14:17], v48 offset:256\n"
  "ds_read_b128 v[18:21], v49 offset:0\n"
  "ds_read_b128 v[22:25], v49 offset:128\n"
  "ds_read_b128 v[26:29], v49 offset:256\n"
  "s_waitcnt lgkmcnt(5)\n"
  "v_smfmac_f32_16x16x64_bf16 v[32:35], v[6:9], a[0:7], v31\n"
  "s_waitcnt lgkmcnt(4)\n"
  "v_smfmac_f32_16x16x64_bf16 v[32:35], v[10:13], a[8:15], v31\n"
  "s_waitcnt lgkmcnt(3)\n"
  "v_smfmac_f32_16x16x64_bf16 v[32:35], v[14:17], a[16:23], v31\n"
  "s_waitcnt lgkmcnt(2)\n"
  "v_smfmac_f32_16x16x64_bf16 v[32:35], v[18:21], a[24:31], v31\n"
  "s_waitcnt lgkmcnt(1)\n"
  "v_smfmac_f32_16x16x64_bf16 v[32:35], v[22:25], a[32:39], v31\n"
  "s_waitcnt lgkmcnt(0)\n"
  "v_smfmac_f32_16x16x64_bf16 v[32:35], v[26:29], a[40:47], v31\n"
  "v_smfmac_f32_16x16x64_bf16 v[36:39], v[6:9], a[48:55], v31\n"
  "v_smfmac_f32_16x16x64_bf16 v[36:39], v[10:13], a[56:63], v31\n"
  "v_smfmac_f32_16x16x64_bf16 v[36:39], v[14:17], a[64:71], v31\n"
  "v_smfmac_f32_16x16x64_bf16 v[36:39], v[18:21], a[72:79], v31\n"
  "v_smfmac_f32_16x16x64_bf16 v[36:39], v[22:25], a[80:87], v31\n"
  "v_smfmac_f32_16x16x64_bf16 v[36:39], v[26:29], a[88:95], v31\n"
  "v_smfmac_f32_16x16x64_bf16 v[40:43], v[6:9], a[96:103], v31\n"
  "v_smfmac_f32_16x16x64_bf16 v[40:43], v[10:13], a[104:111], v31\n"
  "v_smfmac_f32_16x16x64_bf16 v[40:43], v[14:17], a[112:119], v31\n"
  "v_smfmac_f32_16x16x64_bf16 v[40:43], v[18:21], a[120:127], v31\n"
  "v_add_f32 v32, v32, v33\n"
  "v_add_f32 v34, v34, v35\n"
  "v_smfmac_f32_16x16x64_bf16 v[40:43], v[22:25], a[128:135], v31\n"
  "s_nop 0\n"
  "v_add_f32 v36, v36, v37\n"
  "v_add_f32 v38, v38, v39\n"
  "v_smfmac_f32_16x16x64_bf16 v[40:43], v[26:29], a[136:143], v31\n"
  "v_cndmask_b32_e64 v36, v34, v36, s[78:79]\n"
  "v_cndmask_b32_e64 v32, v38, v32, s[78:79]\n"
  "v_smfmac_f32_16x16x64_bf16 v[44:47], v[6:9], a[144:151], v31\n"
  "s_nop 0\n"
  "v_add_f32_dpp v32, v36, v32 row_ror:8 row_mask:0xf bank_mask:0xf\n"
  "v_smfmac_f32_16x16x64_bf16 v[44:47], v[10:13], a[152:159], v31\n"
  "v_smfmac_f32_16x16x64_bf16 v[44:47], v[14:17], a[160:167], v31\n"
  "v_smfmac_f32_16x16x64_bf16 v[44:47], v[18:21], a[168:175], v31\n"
  "v_add_f32 v40, v40, v41\n"
  "v_add_f32 v42, v42, v43\n"
  "v_smfmac_f32_16x16x64_bf16 v[44:47], v[22:25], a[176:183], v31\n"
  "v_smfmac_f32_16x16x64_bf16 v[44:47], v[26:29], a[184:191], v31\n"
  "v_mov_b32 v18, 0\n"
  "v_mov_b32 v19, 0\n"
  "v_mov_b32 v20, 0\n"
  "v_mov_b32 v21, 0\n"
  "v_mov_b32 v22, 0\n"
  "v_mov_b32 v23, 0\n"
  "v_mov_b32 v24, 0\n"
  "v_mov_b32 v25, 0\n"
  "v_add_f32 v44, v44, v45\n"
  "v_add_f32 v46, v46, v47\n"
  "v_cndmask_b32_e64 v44, v42, v44, s[78:79]\n"
  "v_cndmask_b32_e64 v40, v46, v40, s[78:79]\n"
  "s_nop 1\n"
  "v_add_f32_dpp v40, v44, v40 row_ror:8 row_mask:0xf bank_mask:0xf\n"
  "v_max_f32 v33, v32, v40\n"
  "v_mov_b32 v26, 0\n"
  "s_nop 0\n"
  "v_max_f32_dpp v33, v33, v33 quad_perm:[1,0,3,2] row_mask:0xf bank_mask:0xf\n"
  "v_mov_b32 v27, 0\n"
  "s_nop 0\n"
  "v_max_f32_dpp v33, v33, v33 quad_perm:[2,3,0,1] row_mask:0xf bank_mask:0xf\n"
  "v_mov_b32 v28, 0\n"
  "s_nop 0\n"
  "v_max_f32_dpp v33, v33, v33 row_ror:4 row_mask:0xf bank_mask:0xf\n"
  "v_mov_b32 v29, 0\n"
  "s_nop 0\n"
  "v_max_f32_dpp v33, v33, v33 row_ror:8 row_mask:0xf bank_mask:0xf\n"
  "v_mov_b32 v34, v33\n"
  "s_nop 1\n"
  "v_permlane16_swap_b32 v33, v34\n"
  "v_max_f32 v33, v33, v34\n"
  "v_mov_b32 v34, v33\n"
  "s_nop 1\n"
  "v_permlane32_swap_b32 v33, v34\n"
  "v_max_f32 v33, v33, v34\n"
  "v_mul_f32 v39, s80, v33\n"
  "v_fma_f32 v32, v32, s80, -v39\n"
  "v_fma_f32 v40, v40, s80, -v39\n"
  "v_exp_f32 v32, v32\n"
  "v_exp_f32 v40, v40\n"
  "s_nop 0\n"
  "v_cvt_pk_bf16_f32 v33, v32, v40\n"
  "ds_write_b16 v50, v33\n"
  "ds_write_b16_d16_hi v50, v33 offset:128\n"
  "ds_read_b128 a[48:51], v51 offset:0\n"
  "ds_read_b128 a[52:55], v52 offset:0\n"
  "ds_read_b128 a[56:59], v53 offset:0\n"
  "ds_read_b128 a[60:63], v54 offset:0\n"
  "ds_read_b128 a[64:67], v51 offset:128\n"
  "ds_read_b128 a[68:71], v52 offset:128\n"
  "ds_read_b128 a[72:75], v53 offset:128\n"
  "ds_read_b128 a[76:79], v54 offset:128\n"
  "v_add_f32 v38, v32, v40\n"
  "v_lshrrev_b32 v11, 2, v0\n"
  "v_mul_u32_u24 v10, 384, v11\n"
  "v_and_b32 v11, 6, v11\n"
  "v_lshl_add_u32 v10, v11, 4, v10\n"
  "v_and_b32 v11, 3, v0\n"
  "v_lshl_add_u32 v10, v11, 3, v10\n"
  "v_add_u32 v10, 98304, v10\n"
  "s_mul_i32 s70, s33, 3\n"
  "s_add_u32 s70, s70, 0\n"
  "s_and_b32 s71, s70, 3\n"
  "s_lshl_b32 s71, s71, 5\n"
  "s_lshr_b32 s70, s70, 2\n"
  "s_lshl_b32 s70, s70, 7\n"
  "v_xor_b32 v6, s71, v10\n"
  "v_add_u32 v6, s70, v6\n"
  "s_mul_i32 s70, s33, 3\n"
  "s_add_u32 s70, s70, 1\n"
  "s_and_b32 s71, s70, 3\n"
  "s_lshl_b32 s71, s71, 5\n"
  "s_lshr_b32 s70, s70, 2\n"
  "s_lshl_b32 s70, s70, 7\n"
  "v_xor_b32 v7, s71, v10\n"
  "v_add_u32 v7, s70, v7\n"
  "s_mul_i32 s70, s33, 3\n"
  "s_add_u32 s70, s70, 2\n"
  "s_and_b32 s71, s70, 3\n"
  "s_lshl_b32 s71, s71, 5\n"
  "s_lshr_b32 s70, s70, 2\n"
  "s_lshl_b32 s70, s70, 7\n"
  "v_xor_b32 v8, s71, v10\n"
  "v_add_u32 v8, s70, v8\n"
  "s_waitcnt lgkmcnt(7)\n"
  "v_smfmac_f32_16x16x64_bf16 v[18:21], a[48:51], v[56:63], v31\n"
  "v_smfmac_f32_16x16x64_bf16 v[22:25], a[48:51], v[88:95], v31\n"
  "s_mul_i32 s72, s61, 98304\n"
  "s_add_u32 s73, s72, 49536\n"
  "s_mul_i32 s71, s33, 12288\n"
  "s_add_u32 s72, s72, s71\n"
  "s_add_u32 s71, s72, 0\n"
  "buffer_load_dwordx4 a[0:3], v1, s[28:31], s71 offen\n"
  "v_smfmac_f32_16x16x64_bf16 v[26:29], a[48:51], v[120:127], v31\n"
  "v_add_f32_dpp v38, v38, v38 quad_perm:[1,0,3,2] row_mask:0xf bank_mask:0xf\n"
  "s_waitcnt lgkmcnt(6)\n"
  "v_smfmac_f32_16x16x64_bf16 v[18:21], a[52:55], v[64:71], v31\n"
  "buffer_load_dwordx4 a[4:7], v2, s[28:31], s71 offen\n"
  "ds_read_b64_tr_b16 a[96:97], v6 offset:0\n"
  "v_smfmac_f32_16x16x64_bf16 v[22:25], a[52:55], v[96:103], v31\n"
  "ds_read_b64_tr_b16 a[98:99], v6 offset:6144\n"
  "v_smfmac_f32_16x16x64_bf16 v[26:29], a[52:55], v[128:135], v31\n"
  "buffer_load_dwordx4 a[8:11], v3, s[28:31], s71 offen\n"
  "v_add_f32_dpp v38, v38, v38 quad_perm:[2,3,0,1] row_mask:0xf bank_mask:0xf\n"
  "s_waitcnt lgkmcnt(7)\n"
  "v_smfmac_f32_16x16x64_bf16 v[18:21], a[56:59], v[72:79], v31\n"
  "ds_read_b64_tr_b16 a[100:101], v6 offset:12288\n"
  "v_smfmac_f32_16x16x64_bf16 v[22:25], a[56:59], v[104:111], v31\n"
  "s_add_u32 s71, s72, 49152\n"
  "buffer_load_dwordx4 a[12:15], v1, s[28:31], s71 offen\n"
  "ds_read_b64_tr_b16 a[102:103], v6 offset:18432\n"
  "v_smfmac_f32_16x16x64_bf16 v[26:29], a[56:59], v[136:143], v31\n"
  "v_add_f32_dpp v38, v38, v38 row_ror:4 row_mask:0xf bank_mask:0xf\n"
  "s_waitcnt lgkmcnt(8)\n"
  "v_smfmac_f32_16x16x64_bf16 v[18:21], a[60:63], v[80:87], v31\n"
  "buffer_load_dwordx4 a[16:19], v2, s[28:31], s71 offen\n"
  "ds_read_b64_tr_b16 a[104:105], v6 offset:24576\n"
  "v_smfmac_f32_16x16x64_bf16 v[22:25], a[60:63], v[112:119], v31\n"
  "ds_read_b64_tr_b16 a[106:107], v7 offset:0\n"
  "v_smfmac_f32_16x16x64_bf16 v[26:29], a[60:63], v[144:151], v31\n"
  "buffer_load_dwordx4 a[20:23], v3, s[28:31], s71 offen\n"
  "v_add_f32_dpp v38, v38, v38 row_ror:8 row_mask:0xf bank_mask:0xf\n"
  "s_waitcnt lgkmcnt(9)\n"
  "v_smfmac_f32_16x16x64_bf16 v[18:21], a[64:67], v[152:159], v31\n"
  "ds_read_b64_tr_b16 a[108:109], v7 offset:6144\n"
  "v_smfmac_f32_16x16x64_bf16 v[22:25], a[64:67], v[184:191], v31\n"
  "s_add_u32 s71, s72, 384\n"
  "buffer_load_dwordx4 a[24:27], v1, s[28:31], s71 offen\n"
  "ds_read_b64_tr_b16 a[110:111], v7 offset:12288\n"
  "v_smfmac_f32_16x16x64_bf16 v[26:29], a[64:67], a[224:231], v31\n"
  "v_mov_b32 v47, v38\n"
  "s_nop 1\n"
  "v_permlane16_swap_b32 v38, v47\n"
  "v_add_f32 v38, v38, v47\n"
  "s_waitcnt lgkmcnt(10)\n"
  "v_smfmac_f32_16x16x64_bf16 v[18:21], a[68:71], v[160:167], v31\n"
  "buffer_load_dwordx4 a[28:31], v2, s[28:31], s71 offen\n"
  "ds_read_b64_tr_b16 a[112:113], v7 offset:18432\n"
  "v_smfmac_f32_16x16x64_bf16 v[22:25], a[68:71], v[192:199], v31\n"
  "ds_read_b64_tr_b16 a[114:115], v7 offset:24576\n"
  "v_smfmac_f32_16x16x64_bf16 v[26:29], a[68:71], a[232:239], v31\n"
  "buffer_load_dwordx4 a[32:35], v3, s[28:31], s71 offen\n"
  "v_mov_b32 v47, v38\n"
  "s_nop 1\n"
  "v_permlane32_swap_b32 v38, v47\n"
  "v_add_f32 v38, v38, v47\n"
  "s_waitcnt lgkmcnt(11)\n"
  "v_smfmac_f32_16x16x64_bf16 v[18:21], a[72:75], v[168:175], v31\n"
  "ds_read_b64_tr_b16 a[116:117], v8 offset:0\n"
  "v_smfmac_f32_16x16x64_bf16 v[22:25], a[72:75], v[200:207], v31\n"
  "buffer_load_dwordx4 a[36:39], v4, s[28:31], s73 offen\n"
  "ds_read_b64_tr_b16 a[118:119], v8 offset:6144\n"
  "v_smfmac_f32_16x16x64_bf16 v[26:29], a[72:75], a[240:247], v31\n"
  "s_waitcnt lgkmcnt(12)\n"
  "v_smfmac_f32_16x16x64_bf16 v[18:21], a[76:79], v[176:183], v31\n"
  "ds_read_b64_tr_b16 a[120:121], v8 offset:12288\n"
  "v_smfmac_f32_16x16x64_bf16 v[22:25], a[76:79], v[208:215], v31\n"
  "ds_read_b64_tr_b16 a[122:123], v8 offset:18432\n"
  "v_smfmac_f32_16x16x64_bf16 v[26:29], a[76:79], a[248:255], v31\n"
  "ds_read_b64_tr_b16 a[124:125], v8 offset:24576\n"
  "s_lshl_b32 s70, s33, 10\n"
  "v_lshlrev_b32 v40, 4, v0\n"
  "v_add_u32 v40, 30720, v40\n"
  "v_add_u32 v41, s70, v40\n"
  "s_lshl_b32 s70, s33, 2\n"
  "s_add_u32 s70, s70, 162496\n"
  "v_mov_b32 v42, s70\n"
  "v_mov_b32 v43, 162496\n"
  "v_lshlrev_b32 v44, 3, v0\n"
  "s_mov_b32 s66, 2\n"
  "s_mov_b32 s67, 0\n"
  "v_mov_b32 v45, 0x200\n"
  "v_cndmask_b32_e64 v44, v44, v45, s[66:67]\n"
  "s_lshl_b32 s76, s83, 10\n"
  "s_add_u32 s76, s76, 1179648\n"
  "s_waitcnt lgkmcnt(7)\n"
  "ds_write_b32 v42, v38\n"
  "v_add_f32 v36, v18, v19\n"
  "v_add_f32 v37, v22, v23\n"
  "v_add_f32 v38, v26, v27\n"
  "ds_write_b128 v41, v[36:39]\n"
  "s_waitcnt vmcnt(10)\n"
  "s_waitcnt lgkmcnt(0)\n"
  "s_barrier\n"
  "s_cmp_lg_u32 s33, 3\n"
  "s_cbranch_scc1 Lsk_31\n"
  "ds_read_b128 v[6:9], v40 offset:0\n"
  "ds_read_b128 v[10:13], v40 offset:1024\n"
  "ds_read_b128 v[14:17], v40 offset:2048\n"
  "ds_read_b128 v[18:21], v43\n"
  "v_mov_b32 v25, s62\n"
  "s_waitcnt lgkmcnt(0)\n"
  "v_max3_f32 v26, v9, v13, v17\n"
  "v_max_f32 v26, v26, v39\n"
  "v_sub_f32 v9, v9, v26\n"
  "v_sub_f32 v13, v13, v26\n"
  "v_sub_f32 v17, v17, v26\n"
  "v_sub_f32 v39, v39, v26\n"
  "v_exp_f32 v9, v9\n"
  "v_exp_f32 v13, v13\n"
  "v_exp_f32 v17, v17\n"
  "v_exp_f32 v39, v39\n"
  "v_mul_f32 v6, v9, v6\n"
  "v_mul_f32 v7, v9, v7\n"
  "v_mul_f32 v8, v9, v8\n"
  "v_fmac_f32 v6, v13, v10\n"
  "v_fmac_f32 v7, v13, v11\n"
  "v_fmac_f32 v8, v13, v12\n"
  "v_fmac_f32 v6, v17, v14\n"
  "v_fmac_f32 v7, v17, v15\n"
  "v_fmac_f32 v8, v17, v16\n"
  "v_fmac_f32 v6, v39, v36\n"
  "v_fmac_f32 v7, v39, v37\n"
  "v_fmac_f32 v8, v39, v38\n"
  "v_mul_f32 v18, v9, v18\n"
  "v_fmac_f32 v18, v13, v19\n"
  "v_mov_b32_dpp v10, v6 quad_perm:[1,0,3,2] row_mask:0xf bank_mask:0xf\n"
  "v_mov_b32_dpp v11, v7 quad_perm:[1,0,3,2] row_mask:0xf bank_mask:0xf\n"
  "v_mov_b32_dpp v12, v8 quad_perm:[1,0,3,2] row_mask:0xf bank_mask:0xf\n"
  "v_fmac_f32 v18, v17, v20\n"
  "v_fmac_f32 v18, v39, v21\n"
  "v_cvt_pk_bf16_f32 v22, v6, v10\n"
  "v_cvt_pk_bf16_f32 v23, v7, v11\n"
  "v_cvt_pk_bf16_f32 v24, v8, v12\n"
  "v_cndmask_b32_e64 v22, v22, v26, s[66:67]\n"
  "v_cndmask_b32_e64 v23, v23, v18, s[66:67]\n"
  "s_mov_b32 exec_lo, 0x55555557\n"
  "s_mov_b32 exec_hi, 0x55555555\n"
  "buffer_store_dwordx4 v[22:25], v44, s[56:59], s76 offen sc1\n"
  "s_mov_b64 exec, -1\n"
  "Lsk_31:\n"
  "v_lshrrev_b32 v54, 5, v0\n"
  "v_lshlrev_b32 v47, 10, v54\n"
  "v_and_b32 v48, 31, v0\n"
  "v_lshl_add_u32 v46, v48, 4, v47\n"
  "v_add_u32 v47, 0x200, v47\n"
  "s_lshl_b32 s70, s33, 1\n"
  "v_add_u32 v54, s70, v54\n"
  "s_lshl_b32 s71, s34, 3\n"
  "s_add_u32 s70, s70, s71\n"
  "s_lshl_b32 s70, s70, 10\n"
  "s_add_u32 s88, s70, 1179648\n"
  "v_lshlrev_b32 v49, 3, v54\n"
  "v_add_u32 v49, 162432, v49\n"
  "v_mul_u32_u24 v54, 400, v54\n"
  "v_lshl_add_u32 v48, v48, 2, v54\n"
  "v_add_u32 v48, 98304, v48\n"
  "v_and_b32 v50, 7, v0\n"
  "v_mul_u32_u24 v50, 400, v50\n"
  "v_and_b32 v54, 0x30, v0\n"
  "v_add_u32 v50, v50, v54\n"
  "v_add_u32 v50, 98304, v50\n"
  "v_and_b32 v54, 3, v0\n"
  "v_and_b32 v52, 16, v0\n"
  "v_lshrrev_b32 v52, 2, v52\n"
  "v_add_u32 v52, v52, v54\n"
  "v_lshlrev_b32 v52, 3, v52\n"
  "v_add_u32 v52, 162432, v52\n"
  "v_and_b32 v53, 15, v0\n"
  "v_mul_u32_u24 v53, 43, v53\n"
  "v_lshrrev_b32 v53, 8, v53\n"
  "v_lshrrev_b32 v54, 4, v0\n"
  "v_mul_u32_u24 v54, 3, v54\n"
  "s_mul_i32 s71, s33, 6\n"
  "v_add3_u32 v53, v53, v54, s71\n"
  "v_lshrrev_b32 v54, 2, v53\n"
  "v_and_b32 v53, 3, v53\n"
  "v_lshlrev_b32 v53, 4, v53\n"
  "v_lshl_add_u32 v53, v54, 11, v53\n"
  "s_lshl_b32 s70, s35, 7\n"
  "s_add_u32 s70, s70, s34\n"
  "s_lshl_b32 s70, s70, 6\n"
  "s_add_u32 s76, s70, 917504\n"
  "s_cmp_eq_u32 s33, 3\n"
  "s_cbranch_scc1 Lsk_32\n"
  "v_lshrrev_b32 v200, 2, v0\n"
  "v_and_b32 v200, 7, v200\n"
  "v_and_b32 v201, 32, v0\n"
  "v_lshrrev_b32 v201, 1, v201\n"
  "v_add_u32 v200, v200, v201\n"
  "v_mul_u32_u24 v200, 160, v200\n"
  "v_and_b32 v201, 3, v0\n"
  "v_lshl_add_u32 v200, v201, 3, v200\n"
  "s_lshl_b32 s70, s33, 6\n"
  "s_add_u32 s70, s70, 0\n"
  "v_add_u32 v200, s70, v200\n"
  "s_waitcnt lgkmcnt(0)\n"
  "ds_read_b64_tr_b16 a[128:129], v200 offset:0\n"
  "ds_read_b64_tr_b16 a[130:131], v200 offset:1280\n"
  "ds_read_b64_tr_b16 a[132:133], v200 offset:32\n"
  "ds_read_b64_tr_b16 a[134:135], v200 offset:1312\n"
  "ds_read_b64_tr_b16 a[136:137], v200 offset:5120\n"
  "ds_read_b64_tr_b16 a[138:139], v200 offset:6400\n"
  "ds_read_b64_tr_b16 a[140:141], v200 offset:5152\n"
  "ds_read_b64_tr_b16 a[142:143], v200 offset:6432\n"
  "ds_read_b64_tr_b16 a[144:145], v200 offset:10240\n"
  "ds_read_b64_tr_b16 a[146:147], v200 offset:11520\n"
  "ds_read_b64_tr_b16 a[148:149], v200 offset:10272\n"
  "ds_read_b64_tr_b16 a[150:151], v200 offset:11552\n"
  "ds_read_b64_tr_b16 a[152:153], v200 offset:15360\n"
  "ds_read_b64_tr_b16 a[154:155], v200 offset:16640\n"
  "ds_read_b64_tr_b16 a[156:157], v200 offset:15392\n"
  "s_waitcnt lgkmcnt(7)\n"
  "ds_read_b64_tr_b16 a[158:159], v200 offset:16672\n"
  "ds_read_b64_tr_b16 a[160:161], v200 offset:20480\n"
  "ds_read_b64_tr_b16 a[162:163], v200 offset:21760\n"
  "ds_read_b64_tr_b16 a[164:165], v200 offset:20512\n"
  "ds_read_b64_tr_b16 a[166:167], v200 offset:21792\n"
  "ds_read_b64_tr_b16 a[168:169], v200 offset:25600\n"
  "ds_read_b64_tr_b16 a[170:171], v200 offset:26880\n"
  "ds_read_b64_tr_b16 a[172:173], v200 offset:25632\n"
  "s_waitcnt lgkmcnt(7)\n"
  "ds_read_b64_tr_b16 a[174:175], v200 offset:26912\n"
  "s_waitcnt lgkmcnt(0)\n"
  "Lsk_32:\n"
  "LCA2x:\n"
  "s_lshl_b32 s99, s33, 3\n"
  "s_lshr_b32 s98, 0xe0b0b0b, s99\n"
  "s_and_b32 s98, s98, 0xff\n"
  "Ldl_33:\n"
  "s_sub_u32 s98, s98, 1\n"
  "s_cbranch_scc0 Ldl_33\n"
  "buffer_load_dwordx4 v[10:13], v47, s[56:59], s88 offen sc1\n"
  "buffer_load_dwordx4 v[6:9], v46, s[56:59], s88 offen sc1\n"
  "LPA2_34:\n"
  "s_waitcnt vmcnt(1)\n"
  "v_cmp_ne_u32_e64 s[64:65], s62, v13\n"
  "s_waitcnt vmcnt(0)\n"
  "v_cmp_ne_u32 vcc, s62, v9\n"
  "s_nop 0\n"
  "s_or_b64 s[64:65], s[64:65], vcc\n"
  "s_cbranch_scc1 LSA2_35\n"
  "ds_write2_b32 v48, v6, v7 offset0:0 offset1:32\n"
  "ds_write_b32 v48, v8 offset:256\n"
  "ds_write_b64 v49, v[10:11]\n"
  "s_waitcnt lgkmcnt(0)\n"
  "s_barrier\n"
  "s_cmp_eq_u32 s33, 3\n"
  "s_cbranch_scc1 Lsk_36\n"
  "ds_read_b128 v[6:9], v50 offset:0\n"
  "ds_read_b64 v[46:47], v52\n"
  "ds_read_b128 v[10:13], v50 offset:64\n"
  "ds_read_b128 v[14:17], v50 offset:128\n"
  "ds_read_b128 v[18:21], v50 offset:192\n"
  "ds_read_b128 v[22:25], v50 offset:256\n"
  "ds_read_b128 v[26:29], v50 offset:320\n"
  "s_waitcnt lgkmcnt(5)\n"
  "v_mfma_f32_16x16x32_bf16 v[42:45], v[6:9], a[128:131], 0\n"
  "v_mfma_f32_16x16x32_bf16 v[2:5], v[6:9], a[132:135], 0\n"
  "v_mov_b32 v48, v46\n"
  "s_nop 1\n"
  "v_max_f32_dpp v48, v48, v48 quad_perm:[1,0,3,2] row_mask:0xf bank_mask:0xf\n"
  "s_waitcnt lgkmcnt(4)\n"
  "v_mfma_f32_16x16x32_bf16 v[42:45], v[10:13], a[136:139], v[42:45]\n"
  "v_mfma_f32_16x16x32_bf16 v[2:5], v[10:13], a[140:143], v[2:5]\n"
  "v_max_f32_dpp v48, v48, v48 quad_perm:[2,3,0,1] row_mask:0xf bank_mask:0xf\n"
  "v_mov_b32 v49, v48\n"
  "s_nop 1\n"
  "v_permlane16_swap_b32 v48, v49\n"
  "v_max_f32 v48, v48, v49\n"
  "s_waitcnt lgkmcnt(3)\n"
  "v_mfma_f32_16x16x32_bf16 v[42:45], v[14:17], a[144:147], v[42:45]\n"
  "v_mfma_f32_16x16x32_bf16 v[2:5], v[14:17], a[148:151], v[2:5]\n"
  "v_sub_f32 v46, v46, v48\n"
  "v_exp_f32 v46, v46\n"
  "s_nop 0\n"
  "v_mul_f32 v47, v46, v47\n"
  "s_waitcnt lgkmcnt(2)\n"
  "v_mfma_f32_16x16x32_bf16 v[42:45], v[18:21], a[152:155], v[42:45]\n"
  "v_mfma_f32_16x16x32_bf16 v[2:5], v[18:21], a[156:159], v[2:5]\n"
  "v_add_f32_dpp v47, v47, v47 quad_perm:[1,0,3,2] row_mask:0xf bank_mask:0xf\n"
  "s_nop 1\n"
  "v_add_f32_dpp v47, v47, v47 quad_perm:[2,3,0,1] row_mask:0xf bank_mask:0xf\n"
  "s_waitcnt lgkmcnt(1)\n"
  "v_mfma_f32_16x16x32_bf16 v[42:45], v[22:25], a[160:163], v[42:45]\n"
  "v_mfma_f32_16x16x32_bf16 v[2:5], v[22:25], a[164:167], v[2:5]\n"
  "v_mov_b32 v49, v47\n"
  "s_nop 1\n"
  "v_permlane16_swap_b32 v47, v49\n"
  "v_add_f32 v47, v47, v49\n"
  "v_rcp_f32 v47, v47\n"
  "s_waitcnt lgkmcnt(0)\n"
  "v_mfma_f32_16x16x32_bf16 v[42:45], v[26:29], a[168:171], v[42:45]\n"
  "v_mfma_f32_16x16x32_bf16 v[2:5], v[26:29], a[172:175], v[2:5]\n"
  "v_mul_f32 v46, v46, v47\n"
  "v_mov_b32 v9, s62\n"
  "s_cmp_eq_u32 s33, 2\n"
  "s_mov_b32 s71, 0x1041\n"
  "s_cselect_b32 s70, s71, 0x10411041\n"
  "s_nop 1\n"
  "v_mul_f32_dpp v6, v46, v42 quad_perm:[0,0,0,0] row_mask:0xf bank_mask:0xf\n"
  "v_mul_f32_dpp v7, v46, v2 quad_perm:[0,0,0,0] row_mask:0xf bank_mask:0xf\n"
  "s_nop 0\n"
  "v_fmac_f32_dpp v6, v46, v43 quad_perm:[1,1,1,1] row_mask:0xf bank_mask:0xf\n"
  "v_fmac_f32_dpp v7, v46, v3 quad_perm:[1,1,1,1] row_mask:0xf bank_mask:0xf\n"
  "s_nop 0\n"
  "v_fmac_f32_dpp v6, v46, v44 quad_perm:[2,2,2,2] row_mask:0xf bank_mask:0xf\n"
  "v_fmac_f32_dpp v7, v46, v4 quad_perm:[2,2,2,2] row_mask:0xf bank_mask:0xf\n"
  "s_nop 0\n"
  "v_fmac_f32_dpp v6, v46, v45 quad_perm:[3,3,3,3] row_mask:0xf bank_mask:0xf\n"
  "v_fmac_f32_dpp v7, v46, v5 quad_perm:[3,3,3,3] row_mask:0xf bank_mask:0xf\n"
  "s_nop 1\n"
  "v_permlane16_swap_b32 v6, v7\n"
  "v_add_f32 v6, v6, v7\n"
  "s_nop 1\n"
  "v_mov_b32_dpp v7, v6 row_shl:1 row_mask:0xf bank_mask:0xf bound_ctrl:0\n"
  "v_cvt_pk_bf16_f32 v6, v6, v7\n"
  "s_nop 1\n"
  "v_mov_b32_dpp v7, v6 row_shl:2 row_mask:0xf bank_mask:0xf bound_ctrl:0\n"
  "v_mov_b32_dpp v8, v6 row_shl:4 row_mask:0xf bank_mask:0xf bound_ctrl:0\n"
  "s_mov_b32 exec_lo, s70\n"
  "s_mov_b32 exec_hi, 0\n"
  "buffer_store_dwordx4 v[6:9], v53, s[56:59], s76 offen sc1\n"
  "s_mov_b64 exec, -1\n"
  "Lsk_36:\n"
  "s_mul_i32 s70, s33, 3072\n"
  "v_lshlrev_b32 v30, 4, v0\n"
  "v_add_u32 v31, s70, v30\n"
  "s_lshl_b32 s70, s33, 10\n"
  "v_add_u32 v30, s70, v30\n"
  "ds_write_b128 v31, a[0:3] offset:0\n"
  "ds_write_b128 v31, a[4:7] offset:1024\n"
  "ds_write_b128 v31, a[8:11] offset:2048\n"
  "ds_write_b128 v31, a[12:15] offset:12288\n"
  "ds_write_b128 v31, a[16:19] offset:13312\n"
  "ds_write_b128 v31, a[20:23] offset:14336\n"
  "ds_write_b128 v31, a[24:27] offset:24576\n"
  "ds_write_b128 v31, a[28:31] offset:25600\n"
  "ds_write_b128 v31, a[32:35] offset:26624\n"
  "ds_write_b128 v30, a[36:39] offset:36864\n"
  "s_lshl_b32 s70, s33, 2\n"
  "v_and_b32 v2, 3, v0\n"
  "v_add_u32 v2, s70, v2\n"
  "v_min_u32 v3, 14, v2\n"
  "v_sub_u32 v18, v3, v2\n"
  "v_add_u32 v18, v18, v0\n"
  "v_lshlrev_b32 v18, 4, v18\n"
  "s_lshl_b32 s70, s35, 2\n"
  "s_add_u32 s70, s70, s33\n"
  "s_lshl_b32 s70, s70, 11\n"
  "s_add_u32 s88, s70, 917504\n"
  "v_mul_u32_u24 v4, 86, v3\n"
  "v_lshrrev_b32 v4, 8, v4\n"
  "v_mul_u32_u24 v5, 3, v4\n"
  "v_sub_u32 v3, v3, v5\n"
  "v_mul_u32_u24 v3, 3, v3\n"
  "v_lshrrev_b32 v5, 4, v0\n"
  "v_add_u32 v3, v3, v5\n"
  "v_cmp_gt_u32 vcc, 8, v3\n"
  "v_cmp_gt_u32_e64 s[66:67], 15, v2\n"
  "s_nop 0\n"
  "s_and_b64 s[68:69], vcc, s[66:67]\n"
  "s_and_b32 s68, s68, 0x000f000f\n"
  "s_and_b32 s69, s69, 0x000f\n"
  "v_lshl_add_u32 v3, v4, 3, v3\n"
  "v_lshlrev_b32 v3, 2, v3\n"
  "v_add_u32 v19, 160320, v3\n"
  "v_mov_b32 v4, 160480\n"
  "v_cndmask_b32_e64 v19, v4, v19, s[68:69]\n"
  "ds_read_b32 v21, v19\n"
  "v_and_b32 v4, 1, v0\n"
  "v_lshlrev_b32 v4, 4, v4\n"
  "v_mov_b32 v5, 0x3f80\n"
  "v_lshlrev_b32 v4, v4, v5\n"
  "v_mov_b32 v17, 0\n"
  "s_mov_b32 s66, 0x30003\n"
  "s_mov_b32 s67, 0x30003\n"
  "v_cndmask_b32_e64 v14, 0, v4, s[66:67]\n"
  "s_mov_b32 s66, 0x300030\n"
  "s_mov_b32 s67, 0x300030\n"
  "v_cndmask_b32_e64 v15, 0, v4, s[66:67]\n"
  "s_mov_b32 s66, 0x3000300\n"
  "s_mov_b32 s67, 0x3000300\n"
  "v_cndmask_b32_e64 v16, 0, v4, s[66:67]\n"
  "s_waitcnt lgkmcnt(0)\n"
  "v_lshlrev_b32 v20, 16, v21\n"
  "v_and_b32 v21, 0xffff0000, v21\n"
  "s_mov_b32 s66, 0x02100210\n"
  "s_mov_b32 s67, 0x02100210\n"
  "v_lshrrev_b32 v2, 4, v0\n"
  "v_lshlrev_b32 v2, 3, v2\n"
  "v_add_u32 v22, 160320, v2\n"
  "v_add_u32 v2, 160128, v2\n"
  "v_cndmask_b32_e64 v22, v22, v2, s[66:67]\n"
  "s_lshl_b32 s70, s34, 5\n"
  "s_lshl_b32 s71, s33, 3\n"
  "s_add_u32 s70, s70, s71\n"
  "s_add_u32 s70, s70, s35\n"
  "s_lshl_b32 s71, s70, 4\n"
  "s_lshl_b32 s70, s70, 8\n"
  "s_add_u32 s71, s71, 589824\n"
  "v_lshlrev_b32 v54, 4, v0\n"
  "v_add_u32 v54, s70, v54\n"
  "v_add_u32 v2, 327424, v54\n"
  "v_add_u32 v54, 65536, v54\n"
  "s_mov_b32 s66, 0xffff0000\n"
  "s_mov_b32 s67, 0\n"
  "v_cndmask_b32_e64 v54, v54, v2, s[66:67]\n"
  "v_mov_b32 v2, s71\n"
  "s_mov_b32 s66, 0\n"
  "s_mov_b32 s67, -1\n"
  "v_cndmask_b32_e64 v54, v54, v2, s[66:67]\n"
  "LCA3x:\n"
  "s_lshl_b32 s99, s33, 3\n"
  "s_lshr_b32 s98, 0x36181818, s99\n"
  "s_and_b32 s98, s98, 0xff\n"
  "Ldl_37:\n"
  "s_sub_u32 s98, s98, 1\n"
  "s_cbranch_scc0 Ldl_37\n"
  "buffer_load_dwordx4 v[6:9], v18, s[56:59], s88 offen offset:0 sc1\n"
  "buffer_load_dwordx4 v[10:13], v18, s[56:59], s88 offen offset:1024 sc1\n"
  "LPA3_38:\n"
  "s_waitcnt vmcnt(1)\n"
  "v_mfma_f32_16x16x32_bf16 v[2:5], v[14:17], v[6:9], 0\n"
  "v_cmp_ne_u32_e64 s[64:65], s62, v9\n"
  "s_waitcnt vmcnt(0)\n"
  "v_mfma_f32_16x16x32_bf16 v[2:5], v[14:17], v[10:13], v[2:5]\n"
  "v_cmp_ne_u32 vcc, s62, v13\n"
  "s_nop 0\n"
  "s_or_b64 s[64:65], s[64:65], vcc\n"
  "s_cbranch_scc1 LSA3_39\n"
  "s_nop 3\n"
  "v_add_f32_dpp v2, v2, v2 row_ror:4 row_mask:0xf bank_mask:0xf\n"
  "v_add_f32_dpp v3, v3, v3 row_ror:4 row_mask:0xf bank_mask:0xf\n"
  "s_nop 0\n"
  "v_add_f32_dpp v2, v2, v2 row_ror:8 row_mask:0xf bank_mask:0xf\n"
  "v_add_f32_dpp v3, v3, v3 row_ror:8 row_mask:0xf bank_mask:0xf\n"
  "v_cvt_pk_bf16_f32 v3, v2, v3\n"
  "v_lshlrev_b32 v2, 16, v3\n"
  "v_and_b32 v3, 0xffff0000, v3\n"
  "v_add_f32 v2, v20, v2\n"
  "v_add_f32 v3, v21, v3\n"
  "v_cvt_pk_bf16_f32 v2, v2, v3\n"
  "ds_write_b32 v19, v2\n"
  "s_add_u32 s62, s62, 1\n"
  "s_waitcnt lgkmcnt(0)\n"
  "s_barrier\n"
  "ds_read2_b64 v[6:9], v22 offset0:0 offset1:4\n"
  "ds_read2_b64 v[10:13], v22 offset0:8 offset1:12\n"
  "ds_read_b64 v[4:5], v22 offset:128\n"
  "v_mov_b32 v2, 0\n"
  "v_mov_b32 v3, 0\n"
  "s_mov_b32 s66, 0\n"
  "s_mov_b32 s67, -1\n"
  "s_waitcnt lgkmcnt(2)\n"
  "v_mfma_f32_16x16x32_bf16 v[18:21], v[6:9], a[96:99], 0\n"
  "v_mfma_f32_16x16x32_bf16 v[14:17], v[6:9], v[6:9], 0\n"
  "s_waitcnt lgkmcnt(1)\n"
  "v_mfma_f32_16x16x32_bf16 v[18:21], v[10:13], a[100:103], v[18:21]\n"
  "v_mfma_f32_16x16x32_bf16 v[14:17], v[10:13], v[10:13], v[14:17]\n"
  "s_waitcnt lgkmcnt(0)\n"
  "v_mfma_f32_16x16x32_bf16 v[18:21], v[2:5], a[102:105], v[18:21]\n"
  "v_mfma_f32_16x16x32_bf16 v[14:17], v[2:5], v[2:5], v[14:17]\n"
  "v_mfma_f32_16x16x32_bf16 v[24:27], v[6:9], a[106:109], 0\n"
  "v_mfma_f32_16x16x32_bf16 v[24:27], v[10:13], a[110:113], v[24:27]\n"
  "v_mfma_f32_16x16x32_bf16 v[24:27], v[2:5], a[112:115], v[24:27]\n"
  "v_mfma_f32_16x16x32_bf16 v[30:33], v[6:9], a[116:119], 0\n"
  "v_mfma_f32_16x16x32_bf16 v[30:33], v[10:13], a[120:123], v[30:33]\n"
  "v_mfma_f32_16x16x32_bf16 v[30:33], v[2:5], a[122:125], v[30:33]\n"
  "s_nop 1\n"
  "v_cndmask_b32_e64 v28, v18, v14, s[66:67]\n"
  "s_nop 1\n"
  "v_cndmask_b32_e64 v29, v24, v15, s[66:67]\n"
  "s_nop 1\n"
  "v_mov_b32 v31, s62\n"
  "s_mov_b32 exec_lo, 0xffff\n"
  "s_mov_b32 exec_hi, 0x1\n"
  "buffer_store_dwordx4 v[28:31], v54, s[56:59], 0 offen sc1\n"
  "s_mov_b64 exec, -1\n"
  "v_lshlrev_b32 v26, 4, v0\n"
  "v_lshrrev_b32 v27, 1, v0\n"
  "v_and_b32 v27, 7, v27\n"
  "v_lshlrev_b32 v27, 4, v27\n"
  "s_lshl_b32 s70, s34, 2\n"
  "s_add_u32 s70, s70, s33\n"
  "s_lshl_b32 s71, s70, 7\n"
  "s_lshl_b32 s70, s70, 11\n"
  "s_add_u32 s88, s71, 589824\n"
  "s_add_u32 s89, s70, 65536\n"
  "s_add_u32 s90, s70, 327680\n"
  "v_lshrrev_b32 v28, 4, v0\n"
  "v_lshlrev_b32 v28, 1, v28\n"
  "v_mov_b32 v29, 0x98\n"
  "v_lshrrev_b32 v28, v28, v29\n"
  "v_and_b32 v28, 3, v28\n"
  "v_and_b32 v29, 15, v0\n"
  "v_lshl_add_u32 v28, v28, 4, v29\n"
  "s_mul_i32 s70, s33, 48\n"
  "v_add_u32 v28, s70, v28\n"
  "v_lshlrev_b32 v29, 2, v28\n"
  "v_add_u32 v29, 161664, v29\n"
  "v_lshlrev_b32 v28, 1, v28\n"
  "v_add_u32 v28, 160512, v28\n"
  "ds_read_b32 v18, v29\n"
  "s_waitcnt lgkmcnt(0)\n"
  "s_mul_i32 s75, s33, 24576\n"
  "v_and_b32 v206, 15, v0\n"
  "v_mul_u32_u24 v206, 384, v206\n"
  "v_add_u32 v206, s75, v206\n"
  "v_lshrrev_b32 v207, 4, v0\n"
  "v_mul_u32_u24 v207, 3, v207\n"
  "v_and_b32 v208, 6, v0\n"
  "v_add_u32 v200, 0, v207\n"
  "v_xor_b32 v200, v200, v208\n"
  "v_lshl_add_u32 v200, v200, 4, v206\n"
  "v_add_u32 v201, 1, v207\n"
  "v_xor_b32 v201, v201, v208\n"
  "v_lshl_add_u32 v201, v201, 4, v206\n"
  "v_add_u32 v202, 2, v207\n"
  "v_xor_b32 v202, v202, v208\n"
  "v_lshl_add_u32 v202, v202, 4, v206\n"
  "v_add_u32 v203, 12, v207\n"
  "v_xor_b32 v203, v203, v208\n"
  "v_lshl_add_u32 v203, v203, 4, v206\n"
  "v_add_u32 v204, 13, v207\n"
  "v_xor_b32 v204, v204, v208\n"
  "v_lshl_add_u32 v204, v204, 4, v206\n"
  "v_add_u32 v205, 14, v207\n"
  "v_xor_b32 v205, v205, v208\n"
  "v_lshl_add_u32 v205, v205, 4, v206\n"
  "ds_read_b128 v[56:59], v200 offset:0\n"
  "ds_read_b128 v[60:63], v200 offset:6144\n"
  "ds_read_b128 v[64:67], v200 offset:12288\n"
  "ds_read_b128 v[68:71], v200 offset:18432\n"
  "ds_read_b128 v[72:75], v201 offset:0\n"
  "ds_read_b128 v[76:79], v201 offset:6144\n"
  "ds_read_b128 v[80:83], v201 offset:12288\n"
  "ds_read_b128 v[84:87], v201 offset:18432\n"
  "ds_read_b128 v[88:91], v202 offset:0\n"
  "ds_read_b128 v[92:95], v202 offset:6144\n"
  "ds_read_b128 v[96:99], v202 offset:12288\n"
  "ds_read_b128 v[100:103], v202 offset:18432\n"
  "ds_read_b128 v[104:107], v203 offset:0\n"
  "ds_read_b128 v[108:111], v203 offset:6144\n"
  "ds_read_b128 v[112:115], v203 offset:12288\n"
  "s_waitcnt lgkmcnt(7)\n"
  "ds_read_b128 v[116:119], v203 offset:18432\n"
  "ds_read_b128 v[120:123], v204 offset:0\n"
  "ds_read_b128 v[124:127], v204 offset:6144\n"
  "ds_read_b128 v[128:131], v204 offset:12288\n"
  "ds_read_b128 v[132:135], v204 offset:18432\n"
  "ds_read_b128 v[136:139], v205 offset:0\n"
  "ds_read_b128 v[140:143], v205 offset:6144\n"
  "ds_read_b128 v[144:147], v205 offset:12288\n"
  "s_waitcnt lgkmcnt(7)\n"
  "ds_read_b128 v[148:151], v205 offset:18432\n"
  "LCB1x:\n"
  "s_lshl_b32 s99, s33, 3\n"
  "s_lshr_b32 s98, 0x9090909, s99\n"
  "s_and_b32 s98, s98, 0xff\n"
  "Ldl_40:\n"
  "s_sub_u32 s98, s98, 1\n"
  "s_cbranch_scc0 Ldl_40\n"
  "buffer_load_dwordx4 v[6:9], v27, s[56:59], s88 offen sc1\n"
  "buffer_load_dwordx4 v[10:13], v26, s[56:59], s89 offen sc1\n"
  "buffer_load_dwordx4 v[14:17], v26, s[56:59], s89 offen offset:1024 sc1\n"
  "v_and_b32 v40, 1, v0\n"
  "v_lshlrev_b32 v40, 6, v40\n"
  "v_and_b32 v41, 0x30, v0\n"
  "v_add_u32 v40, v40, v41\n"
  "v_add_u32 v40, 160512, v40\n"
  "v_and_b32 v41, 1, v0\n"
  "v_lshlrev_b32 v41, 10, v41\n"
  "v_and_b32 v42, 8, v0\n"
  "v_lshl_add_u32 v41, v42, 8, v41\n"
  "v_and_b32 v42, 6, v0\n"
  "v_lshl_add_u32 v41, v42, 1, v41\n"
  "s_lshr_b32 s70, s34, 3\n"
  "s_lshl_b32 s70, s70, 2\n"
  "s_lshr_b32 s71, s35, 1\n"
  "s_add_u32 s70, s70, s71\n"
  "s_mul_i32 s76, s70, 3072\n"
  "s_and_b32 s70, s34, 4\n"
  "s_add_u32 s70, s70, s33\n"
  "s_lshl_b32 s70, s70, 1\n"
  "s_and_b32 s71, s35, 1\n"
  "s_add_u32 s70, s70, s71\n"
  "s_lshl_b32 s70, s70, 2\n"
  "s_and_b32 s71, s34, 3\n"
  "s_add_u32 s70, s70, s71\n"
  "s_lshl_b32 s70, s70, 4\n"
  "s_add_u32 s70, s70, 786432\n"
  "s_add_u32 s76, s76, s70\n"
  "LPB1_41:\n"
  "s_waitcnt vmcnt(2)\n"
  "v_cmp_ne_u32_e64 s[64:65], s62, v9\n"
  "v_add_f32_dpp v6, v6, v6 quad_perm:[2,3,0,1] row_mask:0xf bank_mask:0xf\n"
  "v_add_f32_dpp v7, v7, v7 quad_perm:[2,3,0,1] row_mask:0xf bank_mask:0xf\n"
  "s_nop 0\n"
  "v_add_f32_dpp v6, v6, v6 row_ror:4 row_mask:0xf bank_mask:0xf\n"
  "v_add_f32_dpp v7, v7, v7 row_ror:4 row_mask:0xf bank_mask:0xf\n"
  "s_nop 0\n"
  "v_add_f32_dpp v6, v6, v6 row_ror:8 row_mask:0xf bank_mask:0xf\n"
  "v_add_f32_dpp v7, v7, v7 row_ror:8 row_mask:0xf bank_mask:0xf\n"
  "v_mul_f32 v7, 0x3acccccd, v7\n"
  "v_mul_f32 v8, v7, v7\n"
  "v_mov_b32 v30, 0x3acccccd\n"
  "v_fma_f32 v6, v6, v30, -v8\n"
  "v_max_f32 v6, 0, v6\n"
  "v_add_f32 v6, 0x3727c5ac, v6\n"
  "v_rsq_f32 v6, v6\n"
  "v_mul_f32 v7, v7, v18\n"
  "s_waitcnt vmcnt(1)\n"
  "v_cmp_ne_u32 vcc, s62, v13\n"
  "s_nop 0\n"
  "s_or_b64 s[64:65], s[64:65], vcc\n"
  "s_waitcnt vmcnt(0)\n"
  "v_cmp_ne_u32 vcc, s62, v17\n"
  "s_nop 0\n"
  "s_or_b64 s[64:65], s[64:65], vcc\n"
  "s_cbranch_scc1 LSB1_42\n"
  "v_add_f32 v10, v10, v14\n"
  "v_add_f32 v11, v11, v15\n"
  "v_add_f32 v12, v12, v16\n"
  "v_mov_b32 v30, v12\n"
  "v_permlane32_swap_b32 v10, v11\n"
  "s_nop 0\n"
  "v_permlane32_swap_b32 v12, v30\n"
  "v_add_f32 v10, v10, v11\n"
  "v_add_f32 v12, v12, v30\n"
  "s_nop 1\n"
  "v_permlane16_swap_b32 v10, v12\n"
  "v_add_f32 v10, v10, v12\n"
  "v_sub_f32 v10, v10, v7\n"
  "v_mul_f32 v10, v10, v6\n"
  "v_cvt_pk_bf16_f32 v10, v10, v10\n"
  "ds_write_b16 v28, v10\n"
  "s_waitcnt lgkmcnt(0)\n"
  "s_barrier\n"
  "ds_read_b128 v[6:9], v40 offset:0\n"
  "ds_read_b128 v[10:13], v40 offset:128\n"
  "ds_read_b128 v[14:17], v40 offset:256\n"
  "s_waitcnt lgkmcnt(2)\n"
  "v_mfma_f32_16x16x32_bf16 v[18:21], v[6:9], a[192:195], 0\n"
  "v_mfma_f32_16x16x32_bf16 v[22:25], v[6:9], a[204:207], 0\n"
  "v_mfma_f32_16x16x32_bf16 v[26:29], v[6:9], a[216:219], 0\n"
  "s_waitcnt lgkmcnt(1)\n"
  "v_mfma_f32_16x16x32_bf16 v[18:21], v[10:13], a[196:199], v[18:21]\n"
  "v_mfma_f32_16x16x32_bf16 v[22:25], v[10:13], a[208:211], v[22:25]\n"
  "v_mfma_f32_16x16x32_bf16 v[26:29], v[10:13], a[220:223], v[26:29]\n"
  "s_waitcnt lgkmcnt(0)\n"
  "v_mfma_f32_16x16x32_bf16 v[18:21], v[14:17], a[200:203], v[18:21]\n"
  "v_mfma_f32_16x16x32_bf16 v[22:25], v[14:17], a[212:215], v[22:25]\n"
  "v_mfma_f32_16x16x32_bf16 v[26:29], v[14:17], v[216:219], v[26:29]\n"
  "s_mov_b32 vcc_lo, 0xaaaaaaaa\n"
  "s_mov_b32 vcc_hi, 0xaaaaaaaa\n"
  "s_nop 3\n"
  "v_add_f32_dpp v18, v19, v18 row_ror:8 row_mask:0xf bank_mask:0xf\n"
  "v_add_f32_dpp v22, v23, v22 row_ror:8 row_mask:0xf bank_mask:0xf\n"
  "s_nop 0\n"
  "v_add_f32_dpp v18, v26, v27 row_ror:8 row_mask:0xf bank_mask:0xc\n"
  "v_mov_b32_dpp v23, v22 quad_perm:[1,0,3,2] row_mask:0xf bank_mask:0xf\n"
  "s_nop 0\n"
  "v_mov_b32_dpp v19, v18 quad_perm:[1,0,3,2] row_mask:0xf bank_mask:0xf\n"
  "v_cvt_pk_bf16_f32 v31, v23, v22\n"
  "v_cvt_pk_bf16_f32 v30, v18, v19\n"
  "v_cndmask_b32 v30, v30, v31, vcc\n"
  "s_mov_b32 exec_lo, 0x55ff\n"
  "s_mov_b32 exec_hi, 0x0\n"
  "buffer_store_dword v30, v41, s[56:59], s76 offen sc1\n"
  "s_mov_b64 exec, -1\n"
  "v_and_b32 v1, 3, v0\n"
  "v_lshlrev_b32 v1, 4, v1\n"
  "v_and_b32 v54, 0x38, v0\n"
  "v_lshl_add_u32 v1, v54, 3, v1\n"
  "v_and_b32 v54, 4, v0\n"
  "v_lshl_add_u32 v1, v54, 7, v1\n"
  "s_lshl_b32 s70, s60, 2\n"
  "s_add_u32 s70, s70, s33\n"
  "s_mul_i32 s70, s70, 3072\n"
  "s_add_u32 s88, s70, 786432\n"
  "s_mul_i32 s75, s33, 24576\n"
  "v_lshrrev_b32 v214, 2, v0\n"
  "v_mul_u32_u24 v210, 384, v214\n"
  "v_and_b32 v214, 6, v214\n"
  "v_lshl_add_u32 v210, v214, 4, v210\n"
  "v_and_b32 v214, 3, v0\n"
  "v_lshl_add_u32 v210, v214, 3, v210\n"
  "v_add_u32 v210, s75, v210\n"
  "v_xor_b32 v211, 32, v210\n"
  "v_xor_b32 v212, 64, v210\n"
  "v_xor_b32 v213, 96, v210\n"
  "ds_read_b64_tr_b16 a[0:1], v210 offset:0\n"
  "ds_read_b64_tr_b16 a[2:3], v210 offset:6144\n"
  "ds_read_b64_tr_b16 a[4:5], v211 offset:0\n"
  "ds_read_b64_tr_b16 a[6:7], v211 offset:6144\n"
  "ds_read_b64_tr_b16 a[8:9], v212 offset:0\n"
  "ds_read_b64_tr_b16 a[10:11], v212 offset:6144\n"
  "ds_read_b64_tr_b16 a[12:13], v213 offset:0\n"
  "ds_read_b64_tr_b16 a[14:15], v213 offset:6144\n"
  "ds_read_b64_tr_b16 a[16:17], v210 offset:128\n"
  "ds_read_b64_tr_b16 a[18:19], v210 offset:6272\n"
  "ds_read_b64_tr_b16 a[20:21], v211 offset:128\n"
  "ds_read_b64_tr_b16 a[22:23], v211 offset:6272\n"
  "ds_read_b64_tr_b16 a[24:25], v212 offset:12416\n"
  "ds_read_b64_tr_b16 a[26:27], v212 offset:18560\n"
  "ds_read_b64_tr_b16 a[28:29], v213 offset:12416\n"
  "s_waitcnt lgkmcnt(7)\n"
  "ds_read_b64_tr_b16 a[30:31], v213 offset:18560\n"
  "ds_read_b64_tr_b16 a[32:33], v210 offset:12544\n"
  "ds_read_b64_tr_b16 a[34:35], v210 offset:18688\n"
  "ds_read_b64_tr_b16 a[36:37], v211 offset:12544\n"
  "ds_read_b64_tr_b16 a[38:39], v211 offset:18688\n"
  "ds_read_b64_tr_b16 a[40:41], v212 offset:12544\n"
  "ds_read_b64_tr_b16 a[42:43], v212 offset:18688\n"
  "ds_read_b64_tr_b16 a[44:45], v213 offset:12544\n"
  "s_waitcnt lgkmcnt(7)\n"
  "ds_read_b64_tr_b16 a[46:47], v213 offset:18688\n"
  "ds_read_b64_tr_b16 a[48:49], v212 offset:128\n"
  "ds_read_b64_tr_b16 a[50:51], v212 offset:6272\n"
  "ds_read_b64_tr_b16 a[52:53], v213 offset:128\n"
  "ds_read_b64_tr_b16 a[54:55], v213 offset:6272\n"
  "ds_read_b64_tr_b16 a[56:57], v210 offset:256\n"
  "ds_read_b64_tr_b16 a[58:59], v210 offset:6400\n"
  "ds_read_b64_tr_b16 a[60:61], v211 offset:256\n"
  "s_waitcnt lgkmcnt(7)\n"
  "ds_read_b64_tr_b16 a[62:63], v211 offset:6400\n"
  "ds_read_b64_tr_b16 a[64:65], v212 offset:256\n"
  "ds_read_b64_tr_b16 a[66:67], v212 offset:6400\n"
  "ds_read_b64_tr_b16 a[68:69], v213 offset:256\n"
  "ds_read_b64_tr_b16 a[70:71], v213 offset:6400\n"
  "ds_read_b64_tr_b16 a[72:73], v210 offset:12288\n"
  "ds_read_b64_tr_b16 a[74:75], v210 offset:18432\n"
  "ds_read_b64_tr_b16 a[76:77], v211 offset:12288\n"
  "s_waitcnt lgkmcnt(7)\n"
  "ds_read_b64_tr_b16 a[78:79], v211 offset:18432\n"
  "ds_read_b64_tr_b16 a[80:81], v212 offset:12288\n"
  "ds_read_b64_tr_b16 a[82:83], v212 offset:18432\n"
  "ds_read_b64_tr_b16 a[84:85], v213 offset:12288\n"
  "ds_read_b64_tr_b16 a[86:87], v213 offset:18432\n"
  "ds_read_b64_tr_b16 a[88:89], v210 offset:12416\n"
  "ds_read_b64_tr_b16 a[90:91], v210 offset:18560\n"
  "ds_read_b64_tr_b16 a[92:93], v211 offset:12416\n"
  "s_waitcnt lgkmcnt(7)\n"
  "ds_read_b64_tr_b16 a[94:95], v211 offset:18560\n"
  "LCB2x:\n"
  "s_lshl_b32 s99, s33, 3\n"
  "s_lshr_b32 s98, 0x1010101, s99\n"
  "s_and_b32 s98, s98, 0xff\n"
  "Ldl_43:\n"
  "s_sub_u32 s98, s98, 1\n"
  "s_cbranch_scc0 Ldl_43\n"
  "buffer_load_dwordx4 v[6:9], v1, s[56:59], s88 offen offset:0 sc1\n"
  "buffer_load_dwordx4 v[22:25], v1, s[56:59], s88 offen offset:1024 sc1\n"
  "buffer_load_dwordx4 v[14:17], v1, s[56:59], s88 offen offset:2048 sc1\n"
  "s_lshl_b32 s70, s60, 5\n"
  "s_add_u32 s70, s70, s33\n"
  "s_lshl_b32 s70, s70, 6\n"
  "s_add_u32 s70, s70, s61\n"
  "s_mul_i32 s70, s70, 512\n"
  "s_add_u32 s93, s70, 8388608\n"
  "s_lshl_b32 s70, s60, 9\n"
  "s_add_u32 s70, s70, s61\n"
  "s_lshl_b32 s70, s70, 4\n"
  "s_add_u32 s94, s70, 884736\n"
  "v_and_b32 v52, 7, v0\n"
  "v_mul_u32_u24 v52, 272, v52\n"
  "v_and_b32 v54, 0x30, v0\n"
  "v_add_u32 v52, v52, v54\n"
  "v_add_u32 v52, 103808, v52\n"
  "v_and_b32 v54, 8, v0\n"
  "v_lshl_add_u32 v53, v54, 4, v52\n"
  "s_mul_i32 s70, s33, 7680\n"
  "v_add_u32 v52, s70, v52\n"
  "LPB2_44:\n"
  "s_waitcnt vmcnt(2)\n"
  "v_max3_u32 v10, v6, v6, v7\n"
  "v_max3_u32 v10, v10, v8, v9\n"
  "s_waitcnt vmcnt(1)\n"
  "v_max3_u32 v10, v10, v22, v23\n"
  "v_max3_u32 v10, v10, v24, v25\n"
  "s_waitcnt vmcnt(0)\n"
  "v_max3_u32 v10, v10, v14, v15\n"
  "v_max3_u32 v10, v10, v16, v17\n"
  "v_cmp_eq_u32 vcc, -1, v10\n"
  "s_nop 0\n"
  "s_or_b64 s[64:65], vcc, vcc\n"
  "s_cbranch_scc1 LSB2_45\n"
  "s_waitcnt lgkmcnt(0)\n"
  "v_mfma_f32_16x16x32_bf16 v[10:13], v[56:59], v[6:9], 0\n"
  "v_mfma_f32_16x16x32_bf16 v[18:21], v[60:63], v[6:9], 0\n"
  "v_mfma_f32_16x16x32_bf16 v[26:29], v[64:67], v[6:9], 0\n"
  "v_mfma_f32_16x16x32_bf16 v[30:33], v[68:71], v[6:9], 0\n"
  "v_mfma_f32_16x16x32_bf16 v[10:13], v[72:75], v[22:25], v[10:13]\n"
  "v_mov_b32_dpp v6, v6 row_ror:8 row_mask:0xf bank_mask:0xf\n"
  "v_mfma_f32_16x16x32_bf16 v[18:21], v[76:79], v[22:25], v[18:21]\n"
  "v_mov_b32_dpp v7, v7 row_ror:8 row_mask:0xf bank_mask:0xf\n"
  "v_mfma_f32_16x16x32_bf16 v[26:29], v[80:83], v[22:25], v[26:29]\n"
  "v_mov_b32_dpp v8, v8 row_ror:8 row_mask:0xf bank_mask:0xf\n"
  "v_mfma_f32_16x16x32_bf16 v[30:33], v[84:87], v[22:25], v[30:33]\n"
  "v_mov_b32_dpp v9, v9 row_ror:8 row_mask:0xf bank_mask:0xf\n"
  "v_mfma_f32_16x16x32_bf16 v[10:13], v[88:91], v[14:17], v[10:13]\n"
  "v_mov_b32_dpp v22, v22 row_ror:8 row_mask:0xf bank_mask:0xf\n"
  "v_mfma_f32_16x16x32_bf16 v[18:21], v[92:95], v[14:17], v[18:21]\n"
  "v_mov_b32_dpp v23, v23 row_ror:8 row_mask:0xf bank_mask:0xf\n"
  "v_mfma_f32_16x16x32_bf16 v[26:29], v[96:99], v[14:17], v[26:29]\n"
  "v_mov_b32_dpp v24, v24 row_ror:8 row_mask:0xf bank_mask:0xf\n"
  "v_mfma_f32_16x16x32_bf16 v[30:33], v[100:103], v[14:17], v[30:33]\n"
  "v_mov_b32_dpp v25, v25 row_ror:8 row_mask:0xf bank_mask:0xf\n"
  "v_mfma_f32_16x16x32_bf16 v[10:13], v[104:107], v[6:9], v[10:13]\n"
  "v_mov_b32_dpp v14, v14 row_ror:8 row_mask:0xf bank_mask:0xf\n"
  "v_mfma_f32_16x16x32_bf16 v[18:21], v[108:111], v[6:9], v[18:21]\n"
  "v_mov_b32_dpp v15, v15 row_ror:8 row_mask:0xf bank_mask:0xf\n"
  "v_mfma_f32_16x16x32_bf16 v[26:29], v[112:115], v[6:9], v[26:29]\n"
  "v_mov_b32_dpp v16, v16 row_ror:8 row_mask:0xf bank_mask:0xf\n"
  "v_mfma_f32_16x16x32_bf16 v[30:33], v[116:119], v[6:9], v[30:33]\n"
  "v_mov_b32_dpp v17, v17 row_ror:8 row_mask:0xf bank_mask:0xf\n"
  "v_mfma_f32_16x16x32_bf16 v[10:13], v[120:123], v[22:25], v[10:13]\n"
  "v_mfma_f32_16x16x32_bf16 v[18:21], v[124:127], v[22:25], v[18:21]\n"
  "v_mfma_f32_16x16x32_bf16 v[26:29], v[128:131], v[22:25], v[26:29]\n"
  "v_mfma_f32_16x16x32_bf16 v[30:33], v[132:135], v[22:25], v[30:33]\n"
  "v_mfma_f32_16x16x32_bf16 v[10:13], v[136:139], v[14:17], v[10:13]\n"
  "v_mfma_f32_16x16x32_bf16 v[18:21], v[140:143], v[14:17], v[18:21]\n"
  "v_mfma_f32_16x16x32_bf16 v[26:29], v[144:147], v[14:17], v[26:29]\n"
  "v_mfma_f32_16x16x32_bf16 v[30:33], v[148:151], v[14:17], v[30:33]\n"
  "s_mov_b64 exec, s[78:79]\n"
  "s_nop 3\n"
  "ds_write_b128 v52, v[10:13] offset:0\n"
  "ds_write_b128 v52, v[18:21] offset:64\n"
  "ds_write_b128 v52, v[26:29] offset:128\n"
  "ds_write_b128 v52, v[30:33] offset:192\n"
  "s_mov_b64 exec, -1\n"
  "s_waitcnt lgkmcnt(0)\n"
  "s_barrier\n"
  "ds_read_b128 v[6:9], v53 offset:0\n"
  "ds_read_b128 v[10:13], v53 offset:64\n"
  "ds_read_b128 v[14:17], v53 offset:7680\n"
  "ds_read_b128 v[18:21], v53 offset:7744\n"
  "ds_read_b128 v[22:25], v53 offset:15360\n"
  "ds_read_b128 v[26:29], v53 offset:15424\n"
  "ds_read_b128 v[30:33], v53 offset:23040\n"
  "ds_read_b128 v[34:37], v53 offset:23104\n"
  "s_waitcnt lgkmcnt(4)\n"
  "v_add_f32 v6, v6, v14\n"
  "v_add_f32 v7, v7, v15\n"
  "v_add_f32 v8, v8, v16\n"
  "v_add_f32 v9, v9, v17\n"
  "v_add_f32 v10, v10, v18\n"
  "v_add_f32 v11, v11, v19\n"
  "v_add_f32 v12, v12, v20\n"
  "v_add_f32 v13, v13, v21\n"
  "s_waitcnt lgkmcnt(2)\n"
  "v_add_f32 v6, v6, v22\n"
  "v_add_f32 v7, v7, v23\n"
  "v_add_f32 v8, v8, v24\n"
  "v_add_f32 v9, v9, v25\n"
  "v_add_f32 v10, v10, v26\n"
  "v_add_f32 v11, v11, v27\n"
  "v_add_f32 v12, v12, v28\n"
  "v_add_f32 v13, v13, v29\n"
  "s_waitcnt lgkmcnt(0)\n"
  "v_add_f32 v6, v6, v30\n"
  "v_add_f32 v7, v7, v31\n"
  "v_add_f32 v8, v8, v32\n"
  "v_add_f32 v9, v9, v33\n"
  "v_add_f32 v10, v10, v34\n"
  "v_add_f32 v11, v11, v35\n"
  "v_add_f32 v12, v12, v36\n"
  "v_add_f32 v13, v13, v37\n"
  "v_max3_f32 v50, v6, v7, v8\n"
  "v_max3_f32 v51, v9, v10, v11\n"
  "v_max3_f32 v50, v50, v12, v13\n"
  "v_max_f32 v50, v50, v51\n"
  "s_nop 1\n"
  "v_max_f32_dpp v50, v50, v50 row_ror:8 row_mask:0xf bank_mask:0xf\n"
  "v_mov_b32 v51, v50\n"
  "s_nop 1\n"
  "v_permlane16_swap_b32 v50, v51\n"
  "v_max_f32 v50, v50, v51\n"
  "v_mov_b32 v51, v50\n"
  "s_nop 1\n"
  "v_permlane32_swap_b32 v50, v51\n"
  "v_max_f32 v50, v50, v51\n"
  "v_mul_f32 v50, s80, v50\n"
  "v_fma_f32 v14, v6, s80, -v50\n"
  "v_fma_f32 v15, v7, s80, -v50\n"
  "v_fma_f32 v16, v8, s80, -v50\n"
  "v_fma_f32 v17, v9, s80, -v50\n"
  "v_fma_f32 v18, v10, s80, -v50\n"
  "v_fma_f32 v19, v11, s80, -v50\n"
  "v_fma_f32 v20, v12, s80, -v50\n"
  "v_fma_f32 v21, v13, s80, -v50\n"
  "v_exp_f32 v14, v14\n"
  "v_exp_f32 v15, v15\n"
  "v_exp_f32 v16, v16\n"
  "v_exp_f32 v17, v17\n"
  "v_exp_f32 v18, v18\n"
  "v_exp_f32 v19, v19\n"
  "v_exp_f32 v20, v20\n"
  "v_exp_f32 v21, v21\n"
  "v_cvt_pk_bf16_f32 v10, v14, v15\n"
  "v_cvt_pk_bf16_f32 v11, v16, v17\n"
  "v_cvt_pk_bf16_f32 v12, v18, v19\n"
  "v_cvt_pk_bf16_f32 v13, v20, v21\n"
  "v_cndmask_b32_e64 v6, 0, v10, s[78:79]\n"
  "v_cndmask_b32_e64 v7, 0, v11, s[78:79]\n"
  "v_cndmask_b32_e64 v8, 0, v12, s[78:79]\n"
  "v_cndmask_b32_e64 v9, 0, v13, s[78:79]\n"
  "v_add_f32 v51, v14, v15\n"
  "v_add_f32 v52, v16, v17\n"
  "v_add_f32 v53, v18, v19\n"
  "v_add_f32 v54, v20, v21\n"
  "v_mfma_f32_16x16x32_bf16 v[14:17], v[6:9], a[0:3], 0\n"
  "v_add_f32 v51, v51, v52\n"
  "v_add_f32 v53, v53, v54\n"
  "v_mfma_f32_16x16x32_bf16 v[18:21], v[6:9], a[4:7], 0\n"
  "v_cndmask_b32_e64 v10, v10, 0, s[78:79]\n"
  "v_cndmask_b32_e64 v11, v11, 0, s[78:79]\n"
  "v_mfma_f32_16x16x32_bf16 v[22:25], v[6:9], a[8:11], 0\n"
  "v_cndmask_b32_e64 v12, v12, 0, s[78:79]\n"
  "v_cndmask_b32_e64 v13, v13, 0, s[78:79]\n"
  "v_mfma_f32_16x16x32_bf16 v[26:29], v[6:9], a[12:15], 0\n"
  "v_add_f32 v51, v51, v53\n"
  "v_mfma_f32_16x16x32_bf16 v[30:33], v[6:9], a[16:19], 0\n"
  "s_nop 0\n"
  "v_add_f32_dpp v51, v51, v51 row_ror:8 row_mask:0xf bank_mask:0xf\n"
  "v_mfma_f32_16x16x32_bf16 v[34:37], v[6:9], a[20:23], 0\n"
  "v_mfma_f32_16x16x32_bf16 v[14:17], v[10:13], a[24:27], v[14:17]\n"
  "v_mov_b32_dpp v6, v6 row_ror:8 row_mask:0xf bank_mask:0xf\n"
  "v_mov_b32_dpp v7, v7 row_ror:8 row_mask:0xf bank_mask:0xf\n"
  "v_mfma_f32_16x16x32_bf16 v[18:21], v[10:13], a[28:31], v[18:21]\n"
  "v_mov_b32_dpp v8, v8 row_ror:8 row_mask:0xf bank_mask:0xf\n"
  "v_mov_b32_dpp v9, v9 row_ror:8 row_mask:0xf bank_mask:0xf\n"
  "v_mfma_f32_16x16x32_bf16 v[22:25], v[10:13], a[32:35], v[22:25]\n"
  "v_mov_b32 v52, v51\n"
  "s_nop 1\n"
  "v_permlane16_swap_b32 v51, v52\n"
  "v_add_f32 v51, v51, v52\n"
  "v_mfma_f32_16x16x32_bf16 v[26:29], v[10:13], a[36:39], v[26:29]\n"
  "v_mov_b32 v52, v51\n"
  "s_nop 1\n"
  "v_permlane32_swap_b32 v51, v52\n"
  "v_add_f32 v51, v51, v52\n"
  "v_mfma_f32_16x16x32_bf16 v[30:33], v[10:13], a[40:43], v[30:33]\n"
  "v_mov_b32 v53, s62\n"
  "v_lshlrev_b32 v54, 10, v0\n"
  "s_cmp_lg_u32 s33, 3\n"
  "s_cbranch_scc1 Lsk_46\n"
  "s_mov_b32 exec_lo, 0xff\n"
  "s_mov_b32 exec_hi, 0x0\n"
  "buffer_store_dwordx4 v[50:53], v54, s[56:59], s94 offen sc1\n"
  "s_mov_b64 exec, -1\n"
  "Lsk_46:\n"
  "v_mfma_f32_16x16x32_bf16 v[34:37], v[10:13], a[44:47], v[34:37]\n"
  "v_mfma_f32_16x16x32_bf16 v[14:17], v[6:9], a[48:51], v[14:17]\n"
  "v_mov_b32_dpp v10, v10 row_ror:8 row_mask:0xf bank_mask:0xf\n"
  "v_mov_b32_dpp v11, v11 row_ror:8 row_mask:0xf bank_mask:0xf\n"
  "v_mfma_f32_16x16x32_bf16 v[18:21], v[6:9], a[52:55], v[18:21]\n"
  "v_mov_b32_dpp v12, v12 row_ror:8 row_mask:0xf bank_mask:0xf\n"
  "v_mov_b32_dpp v13, v13 row_ror:8 row_mask:0xf bank_mask:0xf\n"
  "v_mfma_f32_16x16x32_bf16 v[22:25], v[6:9], a[56:59], v[22:25]\n"
  "v_and_b32 v1, 16, v0\n"
  "v_mul_u32_u24 v1, 32768, v1\n"
  "v_mfma_f32_16x16x32_bf16 v[26:29], v[6:9], a[60:63], v[26:29]\n"
  "v_and_b32 v54, 32, v0\n"
  "v_mad_u32_u24 v1, v54, 8, v1\n"
  "v_mfma_f32_16x16x32_bf16 v[30:33], v[6:9], a[64:67], v[30:33]\n"
  "v_and_b32 v54, 15, v0\n"
  "v_mad_u32_u24 v1, v54, 16, v1\n"
  "v_mfma_f32_16x16x32_bf16 v[34:37], v[6:9], a[68:71], v[34:37]\n"
  "v_mfma_f32_16x16x32_bf16 v[14:17], v[10:13], a[72:75], v[14:17]\n"
  "v_mfma_f32_16x16x32_bf16 v[18:21], v[10:13], a[76:79], v[18:21]\n"
  "v_mfma_f32_16x16x32_bf16 v[22:25], v[10:13], a[80:83], v[22:25]\n"
  "v_mfma_f32_16x16x32_bf16 v[26:29], v[10:13], a[84:87], v[26:29]\n"
  "v_mfma_f32_16x16x32_bf16 v[30:33], v[10:13], a[88:91], v[30:33]\n"
  "v_mfma_f32_16x16x32_bf16 v[34:37], v[10:13], a[92:95], v[34:37]\n"
  "v_mov_b32 v49, s62\n"
  "v_mov_b32 v41, s62\n"
  "s_add_u32 s89, s93, 131072\n"
  "v_mov_b32 v45, s62\n"
  "s_add_u32 s90, s93, 262144\n"
  "v_mov_b32 v9, s62\n"
  "s_add_u32 s91, s93, 393216\n"
  "v_cvt_pk_bf16_f32 v46, v14, v18\n"
  "v_cvt_pk_bf16_f32 v47, v22, v26\n"
  "v_cvt_pk_bf16_f32 v48, v30, v34\n"
  "buffer_store_dwordx4 v[46:49], v1, s[56:59], s93 offen sc1\n"
  "v_cvt_pk_bf16_f32 v38, v15, v19\n"
  "v_cvt_pk_bf16_f32 v39, v23, v27\n"
  "v_cvt_pk_bf16_f32 v40, v31, v35\n"
  "buffer_store_dwordx4 v[38:41], v1, s[56:59], s89 offen sc1\n"
  "v_cvt_pk_bf16_f32 v42, v16, v20\n"
  "v_cvt_pk_bf16_f32 v43, v24, v28\n"
  "v_cvt_pk_bf16_f32 v44, v32, v36\n"
  "buffer_store_dwordx4 v[42:45], v1, s[56:59], s90 offen sc1\n"
  "v_cvt_pk_bf16_f32 v6, v17, v21\n"
  "v_cvt_pk_bf16_f32 v7, v25, v29\n"
  "v_cvt_pk_bf16_f32 v8, v33, v37\n"
  "buffer_store_dwordx4 v[6:9], v1, s[56:59], s91 offen sc1\n"
  "s_cmp_eq_u32 s33, 3\n"
  "s_cbranch_scc1 Lsk_47\n"
  "v_lshrrev_b32 v200, 2, v0\n"
  "v_and_b32 v200, 7, v200\n"
  "v_and_b32 v201, 32, v0\n"
  "v_lshrrev_b32 v201, 1, v201\n"
  "v_add_u32 v200, v200, v201\n"
  "v_mul_u32_u24 v200, 160, v200\n"
  "v_and_b32 v201, 3, v0\n"
  "v_lshl_add_u32 v200, v201, 3, v200\n"
  "s_lshl_b32 s70, s33, 6\n"
  "s_add_u32 s70, s70, 129024\n"
  "v_add_u32 v200, s70, v200\n"
  "s_waitcnt lgkmcnt(0)\n"
  "ds_read_b64_tr_b16 a[128:129], v200 offset:0\n"
  "ds_read_b64_tr_b16 a[130:131], v200 offset:1280\n"
  "ds_read_b64_tr_b16 a[132:133], v200 offset:32\n"
  "ds_read_b64_tr_b16 a[134:135], v200 offset:1312\n"
  "ds_read_b64_tr_b16 a[136:137], v200 offset:5120\n"
  "ds_read_b64_tr_b16 a[138:139], v200 offset:6400\n"
  "ds_read_b64_tr_b16 a[140:141], v200 offset:5152\n"
  "ds_read_b64_tr_b16 a[142:143], v200 offset:6432\n"
  "ds_read_b64_tr_b16 a[144:145], v200 offset:10240\n"
  "ds_read_b64_tr_b16 a[146:147], v200 offset:11520\n"
  "ds_read_b64_tr_b16 a[148:149], v200 offset:10272\n"
  "ds_read_b64_tr_b16 a[150:151], v200 offset:11552\n"
  "ds_read_b64_tr_b16 a[152:153], v200 offset:15360\n"
  "ds_read_b64_tr_b16 a[154:155], v200 offset:16640\n"
  "ds_read_b64_tr_b16 a[156:157], v200 offset:15392\n"
  "s_waitcnt lgkmcnt(7)\n"
  "ds_read_b64_tr_b16 a[158:159], v200 offset:16672\n"
  "ds_read_b64_tr_b16 a[160:161], v200 offset:20480\n"
  "ds_read_b64_tr_b16 a[162:163], v200 offset:21760\n"
  "ds_read_b64_tr_b16 a[164:165], v200 offset:20512\n"
  "ds_read_b64_tr_b16 a[166:167], v200 offset:21792\n"
  "ds_read_b64_tr_b16 a[168:169], v200 offset:25600\n"
  "ds_read_b64_tr_b16 a[170:171], v200 offset:26880\n"
  "ds_read_b64_tr_b16 a[172:173], v200 offset:25632\n"
  "s_waitcnt lgkmcnt(7)\n"
  "ds_read_b64_tr_b16 a[174:175], v200 offset:26912\n"
  "s_waitcnt lgkmcnt(0)\n"
  "Lsk_47:\n"
  "v_and_b32 v45, 12, v0\n"
  "v_lshrrev_b32 v46, 4, v0\n"
  "v_add_u32 v45, v45, v46\n"
  "v_lshlrev_b32 v45, 4, v45\n"
  "v_and_b32 v47, 15, v0\n"
  "v_mul_u32_u24 v46, 512, v46\n"
  "v_mad_u32_u24 v46, v47, 16, v46\n"
  "s_lshl_b32 s70, s34, 2\n"
  "s_add_u32 s70, s70, s33\n"
  "s_lshl_b32 s70, s70, 8\n"
  "s_add_u32 s88, s70, 884736\n"
  "s_lshr_b32 s70, s83, 1\n"
  "s_lshl_b32 s70, s70, 2\n"
  "s_add_u32 s70, s70, s33\n"
  "s_mul_i32 s70, s70, 8192\n"
  "s_and_b32 s71, s35, 1\n"
  "s_mul_i32 s71, s71, 256\n"
  "s_add_u32 s70, s70, s71\n"
  "s_add_u32 s89, s70, 8388608\n"
  "s_add_u32 s90, s89, 4096\n"
  "v_and_b32 v47, 1, v0\n"
  "v_lshlrev_b32 v47, 4, v47\n"
  "v_mov_b32 v48, 0xffff\n"
  "v_lshlrev_b32 v47, v47, v48\n"
  "s_mov_b32 s66, 0x30003\n"
  "s_mov_b32 s67, 0x30003\n"
  "v_cndmask_b32_e64 v42, 0, v47, s[66:67]\n"
  "s_mov_b32 s66, 0x300030\n"
  "s_mov_b32 s67, 0x300030\n"
  "v_cndmask_b32_e64 v43, 0, v47, s[66:67]\n"
  "s_mov_b32 s66, 0x3000300\n"
  "s_mov_b32 s67, 0x3000300\n"
  "v_cndmask_b32_e64 v44, 0, v47, s[66:67]\n"
  "v_mov_b32 v29, 0\n"
  "v_mov_b32 v33, 0\n"
  "v_mov_b32 v37, 0\n"
  "v_mov_b32 v41, 0\n"
  "v_lshlrev_b32 v52, 3, v0\n"
  "v_add_u32 v48, 36864, v52\n"
  "s_lshl_b32 s70, s33, 9\n"
  "v_add_u32 v47, s70, v48\n"
  "s_lshl_b32 s70, s33, 3\n"
  "s_add_u32 s70, s70, 162432\n"
  "v_mov_b32 v49, s70\n"
  "s_lshl_b32 s70, s33, 8\n"
  "s_add_u32 s70, s70, 38912\n"
  "v_lshlrev_b32 v50, 2, v0\n"
  "v_add_u32 v50, s70, v50\n"
  "v_and_b32 v51, 0x30, v0\n"
  "v_add_u32 v51, s70, v51\n"
  "s_lshl_b32 s70, s83, 10\n"
  "s_lshl_b32 s71, s33, 7\n"
  "s_add_u32 s70, s70, s71\n"
  "s_add_u32 s76, s70, 1179648\n"
  "LCB3x:\n"
  "s_lshl_b32 s99, s33, 3\n"
  "s_lshr_b32 s98, 0x241f1f1f, s99\n"
  "s_and_b32 s98, s98, 0xff\n"
  "Ldl_48:\n"
  "s_sub_u32 s98, s98, 1\n"
  "s_cbranch_scc0 Ldl_48\n"
  "buffer_load_dwordx4 v[6:9], v45, s[56:59], s88 offen sc1\n"
  "buffer_load_dwordx4 v[10:13], v46, s[56:59], s89 offen offset:0 sc1\n"
  "buffer_load_dwordx4 v[14:17], v46, s[56:59], s89 offen offset:2048 sc1\n"
  "buffer_load_dwordx4 v[18:21], v46, s[56:59], s90 offen offset:0 sc1\n"
  "buffer_load_dwordx4 v[22:25], v46, s[56:59], s90 offen offset:2048 sc1\n"
  "LPB3_49:\n"
  "s_waitcnt vmcnt(4)\n"
  "v_cmp_ne_u32_e64 s[64:65], s62, v9\n"
  "v_mov_b32 v2, v6\n"
  "s_nop 1\n"
  "v_max_f32_dpp v2, v2, v2 row_ror:4 row_mask:0xf bank_mask:0xf\n"
  "s_nop 1\n"
  "v_max_f32_dpp v2, v2, v2 row_ror:8 row_mask:0xf bank_mask:0xf\n"
  "v_mov_b32 v8, v2\n"
  "s_nop 1\n"
  "v_permlane16_swap_b32 v2, v8\n"
  "v_max_f32 v2, v2, v8\n"
  "v_mov_b32 v8, v2\n"
  "s_nop 1\n"
  "v_permlane32_swap_b32 v2, v8\n"
  "v_max_f32 v2, v2, v8\n"
  "v_sub_f32 v6, v6, v2\n"
  "v_exp_f32 v6, v6\n"
  "s_nop 0\n"
  "v_cvt_pk_bf16_f32 v6, v6, v6\n"
  "s_nop 1\n"
  "v_and_b32_dpp v26, v6, v42 row_newbcast:0 row_mask:0xf bank_mask:0xf\n"
  "v_and_b32_dpp v27, v6, v43 row_newbcast:0 row_mask:0xf bank_mask:0xf\n"
  "v_and_b32_dpp v28, v6, v44 row_newbcast:0 row_mask:0xf bank_mask:0xf\n"
  "v_and_b32_dpp v30, v6, v42 row_newbcast:4 row_mask:0xf bank_mask:0xf\n"
  "v_and_b32_dpp v31, v6, v43 row_newbcast:4 row_mask:0xf bank_mask:0xf\n"
  "v_and_b32_dpp v32, v6, v44 row_newbcast:4 row_mask:0xf bank_mask:0xf\n"
  "v_and_b32_dpp v34, v6, v42 row_newbcast:8 row_mask:0xf bank_mask:0xf\n"
  "v_and_b32_dpp v35, v6, v43 row_newbcast:8 row_mask:0xf bank_mask:0xf\n"
  "v_and_b32_dpp v36, v6, v44 row_newbcast:8 row_mask:0xf bank_mask:0xf\n"
  "v_and_b32_dpp v38, v6, v42 row_newbcast:12 row_mask:0xf bank_mask:0xf\n"
  "v_and_b32_dpp v39, v6, v43 row_newbcast:12 row_mask:0xf bank_mask:0xf\n"
  "v_and_b32_dpp v40, v6, v44 row_newbcast:12 row_mask:0xf bank_mask:0xf\n"
  "v_and_b32 v8, 0xffff0000, v6\n"
  "v_mul_f32 v3, v8, v7\n"
  "s_nop 1\n"
  "v_add_f32_dpp v3, v3, v3 row_ror:4 row_mask:0xf bank_mask:0xf\n"
  "s_nop 1\n"
  "v_add_f32_dpp v3, v3, v3 row_ror:8 row_mask:0xf bank_mask:0xf\n"
  "v_mov_b32 v8, v3\n"
  "s_nop 1\n"
  "v_permlane16_swap_b32 v3, v8\n"
  "v_add_f32 v3, v3, v8\n"
  "v_mov_b32 v8, v3\n"
  "s_nop 1\n"
  "v_permlane32_swap_b32 v3, v8\n"
  "v_add_f32 v3, v3, v8\n"
  "s_waitcnt vmcnt(3)\n"
  "v_mfma_f32_16x16x32_bf16 v[6:9], v[26:29], v[10:13], 0\n"
  "v_cmp_ne_u32 vcc, s62, v13\n"
  "s_nop 0\n"
  "s_or_b64 s[64:65], s[64:65], vcc\n"
  "s_waitcnt vmcnt(2)\n"
  "v_mfma_f32_16x16x32_bf16 v[6:9], v[30:33], v[14:17], v[6:9]\n"
  "v_cmp_ne_u32 vcc, s62, v17\n"
  "s_nop 0\n"
  "s_or_b64 s[64:65], s[64:65], vcc\n"
  "s_waitcnt vmcnt(1)\n"
  "v_mfma_f32_16x16x32_bf16 v[6:9], v[34:37], v[18:21], v[6:9]\n"
  "v_cmp_ne_u32 vcc, s62, v21\n"
  "s_nop 0\n"
  "s_or_b64 s[64:65], s[64:65], vcc\n"
  "s_waitcnt vmcnt(0)\n"
  "v_mfma_f32_16x16x32_bf16 v[6:9], v[38:41], v[22:25], v[6:9]\n"
  "v_cmp_ne_u32 vcc, s62, v25\n"
  "s_nop 0\n"
  "s_or_b64 s[64:65], s[64:65], vcc\n"
  "s_cbranch_scc1 LSB3_50\n"
  "ds_write_b64 v49, v[2:3]\n"
  "v_mov_b32 v53, 162432\n"
  "s_nop 1\n"
  "ds_write_b64 v47, v[6:7]\n"
  "s_waitcnt lgkmcnt(0)\n"
  "s_barrier\n"
  "ds_read_b128 v[18:21], v53\n"
  "ds_read_b128 v[22:25], v53 offset:16\n"
  "ds_read_b64 v[10:11], v48 offset:0\n"
  "ds_read_b64 v[12:13], v48 offset:512\n"
  "ds_read_b64 v[14:15], v48 offset:1024\n"
  "ds_read_b64 v[16:17], v48 offset:1536\n"
  "s_waitcnt lgkmcnt(4)\n"
  "v_max3_f32 v2, v18, v20, v22\n"
  "v_max_f32 v2, v2, v24\n"
  "v_sub_f32 v18, v18, v2\n"
  "v_sub_f32 v20, v20, v2\n"
  "v_sub_f32 v22, v22, v2\n"
  "v_sub_f32 v24, v24, v2\n"
  "v_exp_f32 v18, v18\n"
  "v_exp_f32 v20, v20\n"
  "v_exp_f32 v22, v22\n"
  "v_exp_f32 v24, v24\n"
  "s_waitcnt lgkmcnt(0)\n"
  "v_mul_f32 v10, v18, v10\n"
  "v_mul_f32 v11, v18, v11\n"
  "v_fmac_f32 v10, v20, v12\n"
  "v_fmac_f32 v11, v20, v13\n"
  "v_fmac_f32 v10, v22, v14\n"
  "v_fmac_f32 v11, v22, v15\n"
  "v_fmac_f32 v10, v24, v16\n"
  "v_fmac_f32 v11, v24, v17\n"
  "v_cvt_pk_bf16_f32 v10, v10, v11\n"
  "ds_write_b32 v50, v10\n"
  "ds_read_b128 v[26:29], v51 offset:0\n"
  "ds_read_b128 v[30:33], v51 offset:64\n"
  "ds_read_b128 v[34:37], v51 offset:128\n"
  "v_mul_f32 v19, v18, v19\n"
  "v_fmac_f32 v19, v20, v21\n"
  "v_fmac_f32 v19, v22, v23\n"
  "v_fmac_f32 v19, v24, v25\n"
  "v_rcp_f32 v19, v19\n"
  "v_mov_b32 v41, s62\n"
  "s_waitcnt lgkmcnt(2)\n"
  "v_mfma_f32_16x16x32_bf16 v[6:9], v[26:29], v[220:223], 0\n"
  "v_mfma_f32_16x16x32_bf16 v[10:13], v[26:29], v[232:235], 0\n"
  "v_mfma_f32_16x16x32_bf16 v[14:17], v[26:29], v[244:247], 0\n"
  "s_waitcnt lgkmcnt(1)\n"
  "v_mfma_f32_16x16x32_bf16 v[6:9], v[30:33], v[224:227], v[6:9]\n"
  "v_mfma_f32_16x16x32_bf16 v[10:13], v[30:33], v[236:239], v[10:13]\n"
  "v_mfma_f32_16x16x32_bf16 v[14:17], v[30:33], v[248:251], v[14:17]\n"
  "s_waitcnt lgkmcnt(0)\n"
  "v_mfma_f32_16x16x32_bf16 v[6:9], v[34:37], v[228:231], v[6:9]\n"
  "v_mfma_f32_16x16x32_bf16 v[10:13], v[34:37], v[240:243], v[10:13]\n"
  "v_mfma_f32_16x16x32_bf16 v[14:17], v[34:37], v[252:255], v[14:17]\n"
  "s_nop 5\n"
  "v_mul_f32 v6, v19, v6\n"
  "v_mul_f32 v10, v19, v10\n"
  "v_mul_f32 v14, v19, v14\n"
  "v_mov_b32_dpp v7, v6 quad_perm:[1,0,3,2] row_mask:0xf bank_mask:0xf\n"
  "v_mov_b32_dpp v11, v10 quad_perm:[1,0,3,2] row_mask:0xf bank_mask:0xf\n"
  "v_mov_b32_dpp v15, v14 quad_perm:[1,0,3,2] row_mask:0xf bank_mask:0xf\n"
  "v_cvt_pk_bf16_f32 v38, v6, v7\n"
  "v_cvt_pk_bf16_f32 v39, v10, v11\n"
  "v_cvt_pk_bf16_f32 v40, v14, v15\n"
  "s_mov_b32 exec_lo, 0x5555\n"
  "s_mov_b32 exec_hi, 0x0\n"
  "buffer_store_dwordx4 v[38:41], v52, s[56:59], s76 offen sc1\n"
  "s_mov_b64 exec, -1\n"
  "v_lshrrev_b32 v54, 5, v0\n"
  "v_lshlrev_b32 v47, 10, v54\n"
  "v_and_b32 v48, 31, v0\n"
  "v_lshl_add_u32 v46, v48, 4, v47\n"
  "v_add_u32 v47, 0x200, v47\n"
  "s_lshl_b32 s70, s33, 1\n"
  "v_add_u32 v54, s70, v54\n"
  "s_lshl_b32 s71, s34, 3\n"
  "s_add_u32 s70, s70, s71\n"
  "s_lshl_b32 s70, s70, 10\n"
  "s_add_u32 s88, s70, 1179648\n"
  "v_lshlrev_b32 v49, 3, v54\n"
  "v_add_u32 v49, 162432, v49\n"
  "v_mul_u32_u24 v54, 400, v54\n"
  "v_lshl_add_u32 v48, v48, 2, v54\n"
  "v_add_u32 v48, 30720, v48\n"
  "v_and_b32 v50, 7, v0\n"
  "v_mul_u32_u24 v50, 400, v50\n"
  "v_and_b32 v54, 0x30, v0\n"
  "v_add_u32 v50, v50, v54\n"
  "v_add_u32 v50, 30720, v50\n"
  "v_and_b32 v54, 3, v0\n"
  "v_and_b32 v52, 16, v0\n"
  "v_lshrrev_b32 v52, 2, v52\n"
  "v_add_u32 v52, v52, v54\n"
  "v_lshlrev_b32 v52, 3, v52\n"
  "v_add_u32 v52, 162432, v52\n"
  "v_and_b32 v53, 15, v0\n"
  "v_mul_u32_u24 v53, 43, v53\n"
  "v_lshrrev_b32 v53, 8, v53\n"
  "v_lshrrev_b32 v54, 4, v0\n"
  "v_mul_u32_u24 v54, 3, v54\n"
  "s_mul_i32 s71, s33, 6\n"
  "v_add3_u32 v53, v53, v54, s71\n"
  "v_lshrrev_b32 v54, 2, v53\n"
  "v_and_b32 v53, 3, v53\n"
  "v_lshlrev_b32 v53, 4, v53\n"
  "v_lshl_add_u32 v53, v54, 11, v53\n"
  "s_lshl_b32 s70, s35, 7\n"
  "s_add_u32 s70, s70, s34\n"
  "s_lshl_b32 s70, s70, 6\n"
  "s_add_u32 s76, s70, 983040\n"
  "LCB4x:\n"
  "s_lshl_b32 s99, s33, 3\n"
  "s_lshr_b32 s98, 0x16161616, s99\n"
  "s_and_b32 s98, s98, 0xff\n"
  "Ldl_51:\n"
  "s_sub_u32 s98, s98, 1\n"
  "s_cbranch_scc0 Ldl_51\n"
  "buffer_load_dwordx4 v[6:9], v46, s[56:59], s88 offen sc1\n"
  "LPB4_52:\n"
  "s_waitcnt vmcnt(0)\n"
  "v_cmp_ne_u32 vcc, s62, v9\n"
  "s_nop 0\n"
  "s_or_b64 s[64:65], vcc, vcc\n"
  "s_cbranch_scc1 LSB4_53\n"
  "ds_write2_b32 v48, v6, v7 offset0:0 offset1:32\n"
  "ds_write_b32 v48, v8 offset:256\n"
  "s_waitcnt lgkmcnt(0)\n"
  "s_barrier\n"
  "s_cmp_eq_u32 s33, 3\n"
  "s_cbranch_scc1 Lsk_54\n"
  "ds_read_b128 v[6:9], v50 offset:0\n"
  "ds_read_b128 v[10:13], v50 offset:64\n"
  "ds_read_b128 v[14:17], v50 offset:128\n"
  "ds_read_b128 v[18:21], v50 offset:192\n"
  "ds_read_b128 v[22:25], v50 offset:256\n"
  "ds_read_b128 v[26:29], v50 offset:320\n"
  "s_waitcnt lgkmcnt(5)\n"
  "v_mfma_f32_16x16x32_bf16 v[42:45], v[6:9], a[128:131], 0\n"
  "v_mfma_f32_16x16x32_bf16 v[2:5], v[6:9], a[132:135], 0\n"
  "s_waitcnt lgkmcnt(4)\n"
  "v_mfma_f32_16x16x32_bf16 v[42:45], v[10:13], a[136:139], v[42:45]\n"
  "v_mfma_f32_16x16x32_bf16 v[2:5], v[10:13], a[140:143], v[2:5]\n"
  "s_waitcnt lgkmcnt(3)\n"
  "v_mfma_f32_16x16x32_bf16 v[42:45], v[14:17], a[144:147], v[42:45]\n"
  "v_mfma_f32_16x16x32_bf16 v[2:5], v[14:17], a[148:151], v[2:5]\n"
  "s_waitcnt lgkmcnt(2)\n"
  "v_mfma_f32_16x16x32_bf16 v[42:45], v[18:21], a[152:155], v[42:45]\n"
  "v_mfma_f32_16x16x32_bf16 v[2:5], v[18:21], a[156:159], v[2:5]\n"
  "s_waitcnt lgkmcnt(1)\n"
  "v_mfma_f32_16x16x32_bf16 v[42:45], v[22:25], a[160:163], v[42:45]\n"
  "v_mfma_f32_16x16x32_bf16 v[2:5], v[22:25], a[164:167], v[2:5]\n"
  "s_waitcnt lgkmcnt(0)\n"
  "v_mfma_f32_16x16x32_bf16 v[42:45], v[26:29], a[168:171], v[42:45]\n"
  "v_mfma_f32_16x16x32_bf16 v[2:5], v[26:29], a[172:175], v[2:5]\n"
  "v_mov_b32 v9, s62\n"
  "s_cmp_eq_u32 s33, 2\n"
  "s_mov_b32 s71, 0x1041\n"
  "s_cselect_b32 s70, s71, 0x10411041\n"
  "s_nop 2\n"
  "v_add_f32 v6, v42, v43\n"
  "v_add_f32 v10, v44, v45\n"
  "v_add_f32 v7, v2, v3\n"
  "v_add_f32 v11, v4, v5\n"
  "v_add_f32 v6, v6, v10\n"
  "v_add_f32 v7, v7, v11\n"
  "s_nop 1\n"
  "v_permlane16_swap_b32 v6, v7\n"
  "v_add_f32 v6, v6, v7\n"
  "s_nop 1\n"
  "v_mov_b32_dpp v7, v6 row_shl:1 row_mask:0xf bank_mask:0xf bound_ctrl:0\n"
  "v_cvt_pk_bf16_f32 v6, v6, v7\n"
  "s_nop 1\n"
  "v_mov_b32_dpp v7, v6 row_shl:2 row_mask:0xf bank_mask:0xf bound_ctrl:0\n"
  "v_mov_b32_dpp v8, v6 row_shl:4 row_mask:0xf bank_mask:0xf bound_ctrl:0\n"
  "s_mov_b32 exec_lo, s70\n"
  "s_mov_b32 exec_hi, 0\n"
  "buffer_store_dwordx4 v[6:9], v53, s[56:59], s76 offen sc1\n"
  "s_mov_b64 exec, -1\n"
  "Lsk_54:\n"
  "s_cmp_lg_u32 s34, 0\n"
  "s_cbranch_scc1 LEND\n"
  "s_lshl_b32 s70, s33, 2\n"
  "v_and_b32 v2, 3, v0\n"
  "v_add_u32 v2, s70, v2\n"
  "v_min_u32 v3, 14, v2\n"
  "v_sub_u32 v18, v3, v2\n"
  "v_add_u32 v18, v18, v0\n"
  "v_lshlrev_b32 v18, 4, v18\n"
  "s_lshl_b32 s70, s35, 2\n"
  "s_add_u32 s70, s70, s33\n"
  "s_lshl_b32 s70, s70, 11\n"
  "s_add_u32 s88, s70, 983040\n"
  "v_mul_u32_u24 v4, 86, v3\n"
  "v_lshrrev_b32 v4, 8, v4\n"
  "v_mul_u32_u24 v5, 3, v4\n"
  "v_sub_u32 v3, v3, v5\n"
  "v_mul_u32_u24 v3, 3, v3\n"
  "v_lshrrev_b32 v5, 4, v0\n"
  "v_add_u32 v3, v3, v5\n"
  "v_cmp_gt_u32 vcc, 8, v3\n"
  "v_cmp_gt_u32_e64 s[66:67], 15, v2\n"
  "s_nop 0\n"
  "s_and_b64 s[68:69], vcc, s[66:67]\n"
  "s_and_b32 s68, s68, 0x000f000f\n"
  "s_and_b32 s69, s69, 0x000f\n"
  "v_lshl_add_u32 v3, v4, 3, v3\n"
  "v_lshlrev_b32 v3, 2, v3\n"
  "v_add_u32 v19, 160320, v3\n"
  "v_mov_b32 v4, 160480\n"
  "v_cndmask_b32_e64 v19, v4, v19, s[68:69]\n"
  "ds_read_b32 v21, v19\n"
  "v_and_b32 v4, 1, v0\n"
  "v_lshlrev_b32 v4, 4, v4\n"
  "v_mov_b32 v5, 0x3f80\n"
  "v_lshlrev_b32 v4, v4, v5\n"
  "v_mov_b32 v17, 0\n"
  "s_mov_b32 s66, 0x30003\n"
  "s_mov_b32 s67, 0x30003\n"
  "v_cndmask_b32_e64 v14, 0, v4, s[66:67]\n"
  "s_mov_b32 s66, 0x300030\n"
  "s_mov_b32 s67, 0x300030\n"
  "v_cndmask_b32_e64 v15, 0, v4, s[66:67]\n"
  "s_mov_b32 s66, 0x3000300\n"
  "s_mov_b32 s67, 0x3000300\n"
  "v_cndmask_b32_e64 v16, 0, v4, s[66:67]\n"
  "s_waitcnt lgkmcnt(0)\n"
  "v_lshlrev_b32 v20, 16, v21\n"
  "v_and_b32 v21, 0xffff0000, v21\n"
  "v_mov_b32 v19, v3\n"
  "LCB5x:\n"
  "s_lshl_b32 s99, s33, 3\n"
  "s_lshr_b32 s98, 0xf0e0e0e, s99\n"
  "s_and_b32 s98, s98, 0xff\n"
  "Ldl_55:\n"
  "s_sub_u32 s98, s98, 1\n"
  "s_cbranch_scc0 Ldl_55\n"
  "buffer_load_dwordx4 v[6:9], v18, s[56:59], s88 offen offset:0 sc1\n"
  "buffer_load_dwordx4 v[10:13], v18, s[56:59], s88 offen offset:1024 sc1\n"
  "LPB5_56:\n"
  "s_waitcnt vmcnt(1)\n"
  "v_mfma_f32_16x16x32_bf16 v[2:5], v[14:17], v[6:9], 0\n"
  "v_cmp_ne_u32_e64 s[64:65], s62, v9\n"
  "s_waitcnt vmcnt(0)\n"
  "v_mfma_f32_16x16x32_bf16 v[2:5], v[14:17], v[10:13], v[2:5]\n"
  "v_cmp_ne_u32 vcc, s62, v13\n"
  "s_nop 0\n"
  "s_or_b64 s[64:65], s[64:65], vcc\n"
  "s_cbranch_scc1 LSB5_57\n"
  "s_nop 3\n"
  "v_add_f32_dpp v2, v2, v2 row_ror:4 row_mask:0xf bank_mask:0xf\n"
  "v_add_f32_dpp v3, v3, v3 row_ror:4 row_mask:0xf bank_mask:0xf\n"
  "s_nop 0\n"
  "v_add_f32_dpp v2, v2, v2 row_ror:8 row_mask:0xf bank_mask:0xf\n"
  "v_add_f32_dpp v3, v3, v3 row_ror:8 row_mask:0xf bank_mask:0xf\n"
  "v_cvt_pk_bf16_f32 v3, v2, v3\n"
  "v_lshlrev_b32 v2, 16, v3\n"
  "v_and_b32 v3, 0xffff0000, v3\n"
  "v_add_f32 v2, v20, v2\n"
  "v_add_f32 v3, v21, v3\n"
  "v_cvt_pk_bf16_f32 v2, v2, v3\n"
  "s_mov_b64 exec, s[68:69]\n"
  "buffer_store_dword v2, v19, s[52:55], s77 offen\n"
  "s_mov_b64 exec, -1\n"
  "s_add_u32 s70, s35, s33\n"
  "s_cmp_lg_u32 s70, 0\n"
  "s_cbranch_scc1 LEND\n"
  "v_mov_b32 v6, s62\n"
  "v_mov_b32 v7, 0\n"
  "s_mov_b32 exec_lo, 0x1\n"
  "s_mov_b32 exec_hi, 0x0\n"
  "buffer_store_dword v6, v7, s[56:59], 0 offen sc1\n"
  "s_mov_b64 exec, -1\n"
  "LEND:\n"
  "s_endpgm\n"
  "LSA1_2:\n"
  "s_add_u32 s63, s63, 1\n"
  "s_cmp_gt_u32 s63, 200000\n"
  "s_cbranch_scc1 LEND\n"
  "s_sleep 1\n"
  "buffer_load_dwordx4 v[6:9], v27, s[56:59], s88 offen sc1\n"
  "buffer_load_dwordx4 v[18:21], v26, s[56:59], s90 offen sc1\n"
  "buffer_load_dwordx4 v[22:25], v26, s[56:59], s90 offen offset:1024 sc1\n"
  "buffer_load_dwordx4 v[10:13], v26, s[56:59], s89 offen sc1\n"
  "buffer_load_dwordx4 v[14:17], v26, s[56:59], s89 offen offset:1024 sc1\n"
  "s_waitcnt vmcnt(0)\n"
  "s_branch LPA1_1\n"
  "LSA2_6:\n"
  "s_add_u32 s63, s63, 1\n"
  "s_cmp_gt_u32 s63, 200000\n"
  "s_cbranch_scc1 LEND\n"
  "s_sleep 1\n"
  "buffer_load_dwordx4 v[10:13], v47, s[56:59], s88 offen sc1\n"
  "buffer_load_dwordx4 v[6:9], v46, s[56:59], s88 offen sc1\n"
  "s_waitcnt vmcnt(0)\n"
  "s_branch LPA2_5\n"
  "LSA3_10:\n"
  "s_add_u32 s63, s63, 1\n"
  "s_cmp_gt_u32 s63, 200000\n"
  "s_cbranch_scc1 LEND\n"
  "s_sleep 1\n"
  "buffer_load_dwordx4 v[6:9], v18, s[56:59], s88 offen offset:0 sc1\n"
  "buffer_load_dwordx4 v[10:13], v18, s[56:59], s88 offen offset:1024 sc1\n"
  "s_waitcnt vmcnt(0)\n"
  "s_branch LPA3_9\n"
  "LSB1_13:\n"
  "s_add_u32 s63, s63, 1\n"
  "s_cmp_gt_u32 s63, 200000\n"
  "s_cbranch_scc1 LEND\n"
  "s_sleep 1\n"
  "buffer_load_dwordx4 v[6:9], v27, s[56:59], s88 offen sc1\n"
  "buffer_load_dwordx4 v[18:21], v26, s[56:59], s90 offen sc1\n"
  "buffer_load_dwordx4 v[22:25], v26, s[56:59], s90 offen offset:1024 sc1\n"
  "buffer_load_dwordx4 v[10:13], v26, s[56:59], s89 offen sc1\n"
  "buffer_load_dwordx4 v[14:17], v26, s[56:59], s89 offen offset:1024 sc1\n"
  "s_waitcnt vmcnt(0)\n"
  "s_branch LPB1_12\n"
  "LSB2_16:\n"
  "s_add_u32 s63, s63, 1\n"
  "s_cmp_gt_u32 s63, 200000\n"
  "s_cbranch_scc1 LEND\n"
  "s_sleep 1\n"
  "buffer_load_dwordx4 v[6:9], v1, s[56:59], s88 offen offset:0 sc1\n"
  "buffer_load_dwordx4 v[22:25], v1, s[56:59], s88 offen offset:1024 sc1\n"
  "buffer_load_dwordx4 v[14:17], v1, s[56:59], s88 offen offset:2048 sc1\n"
  "s_waitcnt vmcnt(0)\n"
  "s_branch LPB2_15\n"
  "LSB3_20:\n"
  "s_add_u32 s63, s63, 1\n"
  "s_cmp_gt_u32 s63, 200000\n"
  "s_cbranch_scc1 LEND\n"
  "s_sleep 1\n"
  "buffer_load_dwordx4 v[6:9], v45, s[56:59], s88 offen sc1\n"
  "buffer_load_dwordx4 v[10:13], v46, s[56:59], s89 offen offset:0 sc1\n"
  "buffer_load_dwordx4 v[14:17], v46, s[56:59], s89 offen offset:2048 sc1\n"
  "buffer_load_dwordx4 v[18:21], v46, s[56:59], s90 offen offset:0 sc1\n"
  "buffer_load_dwordx4 v[22:25], v46, s[56:59], s90 offen offset:2048 sc1\n"
  "s_waitcnt vmcnt(0)\n"
  "s_branch LPB3_19\n"
  "LSB4_23:\n"
  "s_add_u32 s63, s63, 1\n"
  "s_cmp_gt_u32 s63, 200000\n"
  "s_cbranch_scc1 LEND\n"
  "s_sleep 1\n"
  "buffer_load_dwordx4 v[6:9], v46, s[56:59], s88 offen sc1\n"
  "s_waitcnt vmcnt(0)\n"
  "s_branch LPB4_22\n"
  "LSB5_27:\n"
  "s_add_u32 s63, s63, 1\n"
  "s_cmp_gt_u32 s63, 200000\n"
  "s_cbranch_scc1 LEND\n"
  "s_sleep 1\n"
  "buffer_load_dwordx4 v[6:9], v18, s[56:59], s88 offen offset:0 sc1\n"
  "buffer_load_dwordx4 v[10:13], v18, s[56:59], s88 offen offset:1024 sc1\n"
  "s_waitcnt vmcnt(0)\n"
  "s_branch LPB5_26\n"
  "LSA1_30:\n"
  "s_add_u32 s63, s63, 1\n"
  "s_cmp_gt_u32 s63, 200000\n"
  "s_cbranch_scc1 LEND\n"
  "s_sleep 1\n"
  "buffer_load_dwordx4 v[6:9], v27, s[56:59], s88 offen sc1\n"
  "buffer_load_dwordx4 v[10:13], v26, s[56:59], s89 offen sc1\n"
  "buffer_load_dwordx4 v[14:17], v26, s[56:59], s89 offen offset:1024 sc1\n"
  "s_waitcnt vmcnt(0)\n"
  "s_branch LPA1_29\n"
  "LSA2_35:\n"
  "s_add_u32 s63, s63, 1\n"
  "s_cmp_gt_u32 s63, 200000\n"
  "s_cbranch_scc1 LEND\n"
  "s_sleep 1\n"
  "buffer_load_dwordx4 v[10:13], v47, s[56:59], s88 offen sc1\n"
  "buffer_load_dwordx4 v[6:9], v46, s[56:59], s88 offen sc1\n"
  "s_waitcnt vmcnt(0)\n"
  "s_branch LPA2_34\n"
  "LSA3_39:\n"
  "s_add_u32 s63, s63, 1\n"
  "s_cmp_gt_u32 s63, 200000\n"
  "s_cbranch_scc1 LEND\n"
  "s_sleep 1\n"
  "buffer_load_dwordx4 v[6:9], v18, s[56:59], s88 offen offset:0 sc1\n"
  "buffer_load_dwordx4 v[10:13], v18, s[56:59], s88 offen offset:1024 sc1\n"
  "s_waitcnt vmcnt(0)\n"
  "s_branch LPA3_38\n"
  "LSB1_42:\n"
  "s_add_u32 s63, s63, 1\n"
  "s_cmp_gt_u32 s63, 200000\n"
  "s_cbranch_scc1 LEND\n"
  "s_sleep 1\n"
  "buffer_load_dwordx4 v[6:9], v27, s[56:59], s88 offen sc1\n"
  "buffer_load_dwordx4 v[10:13], v26, s[56:59], s89 offen sc1\n"
  "buffer_load_dwordx4 v[14:17], v26, s[56:59], s89 offen offset:1024 sc1\n"
  "s_waitcnt vmcnt(0)\n"
  "s_branch LPB1_41\n"
  "LSB2_45:\n"
  "s_add_u32 s63, s63, 1\n"
  "s_cmp_gt_u32 s63, 200000\n"
  "s_cbranch_scc1 LEND\n"
  "s_sleep 1\n"
  "buffer_load_dwordx4 v[6:9], v1, s[56:59], s88 offen offset:0 sc1\n"
  "buffer_load_dwordx4 v[22:25], v1, s[56:59], s88 offen offset:1024 sc1\n"
  "buffer_load_dwordx4 v[14:17], v1, s[56:59], s88 offen offset:2048 sc1\n"
  "s_waitcnt vmcnt(0)\n"
  "s_branch LPB2_44\n"
  "LSB3_50:\n"
  "s_add_u32 s63, s63, 1\n"
  "s_cmp_gt_u32 s63, 200000\n"
  "s_cbranch_scc1 LEND\n"
  "s_sleep 1\n"
  "buffer_load_dwordx4 v[6:9], v45, s[56:59], s88 offen sc1\n"
  "buffer_load_dwordx4 v[10:13], v46, s[56:59], s89 offen offset:0 sc1\n"
  "buffer_load_dwordx4 v[14:17], v46, s[56:59], s89 offen offset:2048 sc1\n"
  "buffer_load_dwordx4 v[18:21], v46, s[56:59], s90 offen offset:0 sc1\n"
  "buffer_load_dwordx4 v[22:25], v46, s[56:59], s90 offen offset:2048 sc1\n"
  "s_waitcnt vmcnt(0)\n"
  "s_branch LPB3_49\n"
  "LSB4_53:\n"
  "s_add_u32 s63, s63, 1\n"
  "s_cmp_gt_u32 s63, 200000\n"
  "s_cbranch_scc1 LEND\n"
  "s_sleep 1\n"
  "buffer_load_dwordx4 v[6:9], v46, s[56:59], s88 offen sc1\n"
  "s_waitcnt vmcnt(0)\n"
  "s_branch LPB4_52\n"
  "LSB5_57:\n"
  "s_add_u32 s63, s63, 1\n"
  "s_cmp_gt_u32 s63, 200000\n"
  "s_cbranch_scc1 LEND\n"
  "s_sleep 1\n"
  "buffer_load_dwordx4 v[6:9], v18, s[56:59], s88 offen offset:0 sc1\n"
  "buffer_load_dwordx4 v[10:13], v18, s[56:59], s88 offen offset:1024 sc1\n"
  "s_waitcnt vmcnt(0)\n"
  "s_branch LPB5_56\n"
  : "+v"(tid)
  : "s"(__builtin_amdgcn_kernarg_segment_ptr()), "s"(blockIdx.x),
    "s"((unsigned)(size_t)(__attribute__((address_space(3))) unsigned char*)lds)
  : "v1","v2","v3","v4","v5","v6","v7","v8","v9","v10","v11","v12","v13","v14","v15","v16","v17","v18","v19","v20","v21","v22","v23","v24","v25","v26","v27","v28","v29","v30","v31","v32","v33","v34","v35","v36","v37","v38","v39","v40","v41","v42","v43","v44","v45","v46","v47","v48","v49","v50","v51","v52","v53","v54","v55","v56","v57","v58","v59","v60","v61","v62","v63","v64","v65","v66","v67","v68","v69","v70","v71","v72","v73","v74","v75","v76","v77","v78","v79","v80","v81","v82","v83","v84","v85","v86","v87","v88","v89","v90","v91","v92","v93","v94","v95","v96","v97","v98","v99","v100","v101","v102","v103","v104","v105","v106","v107","v108","v109","v110","v111","v112","v113","v114","v115","v116","v117","v118","v119","v120","v121","v122","v123","v124","v125","v126","v127","v128","v129","v130","v131","v132","v133","v134","v135","v136","v137","v138","v139","v140","v141","v142","v143","v144","v145","v146","v147","v148","v149","v150","v151","v152","v153","v154","v155","v156","v157","v158","v159","v160","v161","v162","v163","v164","v165","v166","v167","v168","v169","v170","v171","v172","v173","v174","v175","v176","v177","v178","v179","v180","v181","v182","v183","v184","v185","v186","v187","v188","v189","v190","v191","v192","v193","v194","v195","v196","v197","v198","v199","v200","v201","v202","v203","v204","v205","v206","v207","v208","v209","v210","v211","v212","v213","v214","v215","v216","v217","v218","v219","v220","v221","v222","v223","v224","v225","v226","v227","v228","v229","v230","v231","v232","v233","v234","v235","v236","v237","v238","v239","v240","v241","v242","v243","v244","v245","v246","v247","v248","v249","v250","v251","v252","v253","v254","v255","a0","a1","a2","a3","a4","a5","a6","a7","a8","a9","a10","a11","a12","a13","a14","a15","a16","a17","a18","a19","a20","a21","a22","a23","a24","a25","a26","a27","a28","a29","a30","a31","a32","a33","a34","a35","a36","a37","a38","a39","a40","a41","a42","a43","a44","a45","a46","a47","a48","a49","a50","a51","a52","a53","a54","a55","a56","a57","a58","a59","a60","a61","a62","a63","a64","a65","a66","a67","a68","a69","a70","a71","a72","a73","a74","a75","a76","a77","a78","a79","a80","a81","a82","a83","a84","a85","a86","a87","a88","a89","a90","a91","a92","a93","a94","a95","a96","a97","a98","a99","a100","a101","a102","a103","a104","a105","a106","a107","a108","a109","a110","a111","a112","a113","a114","a115","a116","a117","a118","a119","a120","a121","a122","a123","a124","a125","a126","a127","a128","a129","a130","a131","a132","a133","a134","a135","a136","a137","a138","a139","a140","a141","a142","a143","a144","a145","a146","a147","a148","a149","a150","a151","a152","a153","a154","a155","a156","a157","a158","a159","a160","a161","a162","a163","a164","a165","a166","a167","a168","a169","a170","a171","a172","a173","a174","a175","a176","a177","a178","a179","a180","a181","a182","a183","a184","a185","a186","a187","a188","a189","a190","a191","a192","a193","a194","a195","a196","a197","a198","a199","a200","a201","a202","a203","a204","a205","a206","a207","a208","a209","a210","a211","a212","a213","a214","a215","a216","a217","a218","a219","a220","a221","a222","a223","a224","a225","a226","a227","a228","a229","a230","a231","a232","a233","a234","a235","a236","a237","a238","a239","a240","a241","a242","a243","a244","a245","a246","a247","a248","a249","a250","a251","a252","a253","a254","a255","s8","s9","s10","s11","s12","s13","s14","s15","s16","s17","s18","s19","s20","s21","s22","s23","s24","s25","s26","s27","s28","s29","s30","s31","s33","s34","s35","s36","s37","s38","s39","s40","s41","s42","s43","s44","s45","s46","s47","s48","s49","s50","s51","s52","s53","s54","s55","s56","s57","s58","s59","s60","s61","s62","s63","s64","s65","s66","s67","s68","s69","s70","s71","s72","s73","s74","s75","s76","s77","s78","s79","s80","s81","s82","s83","s84","s85","s86","s87","s88","s89","s90","s91","s92","s93","s94","s95","s96","s97","s98","s99","vcc","scc","memory");
}

extern "C" void kernel_launch(void* const* d_in, const int* in_sizes, int n_in, void* d_out, int out_size, void* d_ws, size_t ws_size,
                              hipStream_t stream) {
  decode_megakernel<<<256, 256, 0, stream>>>(d_in[0], d_in[1], d_in[3], d_in[2], d_in[4], d_in[6], d_in[7], d_in[8], d_in[9], d_in[10],
                                             d_out, d_ws);
}
```
